```python
import torch

def _softmax(x, axis=-1):
    return torch.softmax(x, dim=axis)
def _gelu(x):
    return torch.nn.functional.gelu(x, approximate="tanh")
def _silu(x):
    return torch.nn.functional.silu(x)
def _sigmoid(x):
    return torch.sigmoid(x)
def _layernorm(x, axis=-1, eps=1e-5):
    m = x.mean(dim=axis, keepdim=True)
    v = x.var(dim=axis, keepdim=True, unbiased=False)
    return (x - m) / torch.sqrt(v + eps)
def _scatter_add(tgt, idx, src, axis=0):
    return tgt.clone().index_add_(axis, idx, src)
def _segment_sum(src, seg, n):
    out = torch.zeros((n,) + src.shape[1:], dtype=src.dtype)
    return out.index_add_(0, seg, src)
def _pad(x, pw):
    flat = tuple(v for pair in reversed(pw) for v in pair)
    return torch.nn.functional.pad(x, flat)

kernel_name = 'standalone__gqa_softmax__decode'

def setup_inputs():

    _w = (lambda a: (lambda t: t.to(torch.bfloat16) if t.is_floating_point() else t)(torch.as_tensor(a)))
    B, Hq, Hkv, Sq, Sk, D = 4, 16, 8, 1, 16384, 256
    return {
        "q": _w((lambda *s: torch.randn(*s, dtype=torch.bfloat16) * 0.8)(B, Hq, Sq, D)),
        "k": _w((lambda *s: torch.randn(*s, dtype=torch.bfloat16) * 0.8)(B, Hkv, Sk, D)),
        "v": _w((lambda *s: torch.randn(*s, dtype=torch.bfloat16) * 0.05)(B, Hkv, Sk, D)),
    }

def reference(q, k, v):
    B, Hq, Sq, D = q.shape
    Hkv = k.shape[1]
    rep = Hq // Hkv
    k = (lambda x, n, axis: torch.repeat_interleave(x, n, dim=axis))(k, rep, axis=1)
    v = (lambda x, n, axis: torch.repeat_interleave(x, n, dim=axis))(v, rep, axis=1)
    scale = D ** -0.5
    scores = torch.einsum("bhqd,bhkd->bhqk", q, k) * scale
    p = _softmax(scores, axis=-1)
    return torch.einsum("bhqk,bhkd->bhqd", p, v)
```

```cpp
#include <hip/hip_runtime.h>
#include <hip/hip_bf16.h>
#include <stdint.h>

#define NSPLIT 8
#define NWAVES 8
#define INTERLEAVE 0
#define GROUP_MAJOR 1

namespace {

constexpr int B = 4, HQ = 16, HKV = 8, G = HQ / HKV, SK = 16384, D = 256;
constexpr int NGROUP = B * HKV;
constexpr int TK = 16;
constexpr int NT = SK / (NSPLIT * NWAVES * TK);
constexpr int NTHR = NWAVES * 64;
static_assert(G == 2, "layout assumes 2 query heads per kv head");
static_assert(NT >= 2 && NT % 2 == 0, "two register sets");

typedef unsigned u32x4 __attribute__((ext_vector_type(4)));
typedef float f32x4 __attribute__((ext_vector_type(4)));
typedef __bf16 bf16x2 __attribute__((ext_vector_type(2)));

__device__ __forceinline__ float dot2(unsigned a, unsigned b, float acc) {
  return __builtin_amdgcn_fdot2_f32_bf16(__builtin_bit_cast(bf16x2, a), __builtin_bit_cast(bf16x2, b), acc, false);
}
template <int CTRL> __device__ __forceinline__ float dpp(float x) {
  return __builtin_bit_cast(float, __builtin_amdgcn_mov_dpp(__builtin_bit_cast(int, x), CTRL, 0xf, 0xf, true));
}
constexpr int XOR1 = 0xB1, XOR2 = 0x4E, XOR7 = 0x141, XOR8 = 0x128, ROR4 = 0x124, BCAST = 0x150;

__device__ __forceinline__ u32x4 ldnt(const unsigned char* p) {
  return __builtin_nontemporal_load(reinterpret_cast<const u32x4*>(p));
}

struct State {
  float m;
  float l;
  float acc[16];
};

template <bool ISSUE>
__device__ __forceinline__ void step(State& st, u32x4 (&kr)[8], u32x4 (&vr)[8], const u32x4 (&q)[2], int lane,
                                     const unsigned char* kn, const unsigned char* vn) {
  constexpr float C2 = 0.0625f * 1.4426950408889634f;
  float x[16];
#pragma unroll
  for (int t = 0; t < 8; ++t)
#pragma unroll
    for (int g = 0; g < 2; ++g) {
      float d = 0.f;
#pragma unroll
      for (int k = 0; k < 4; ++k) d = dot2(kr[t][k], q[g][k], d);
      x[t * 2 + g] = d;
    }
  if (ISSUE) {
    __builtin_amdgcn_sched_barrier(0);
#pragma unroll
    for (int t = 0; t < 8; ++t) kr[t] = ldnt(kn + t * 1024);
    __builtin_amdgcn_sched_barrier(0);
  }
#define TR_STEP(HALF, CTRL, BIT)                                       \
  _Pragma("unroll") for (int i = 0; i < (HALF); ++i) {                 \
    const float lo_ = x[i], hi_ = x[i + (HALF)];                       \
    const float t1_ = lo_ + dpp<CTRL>(lo_), t2_ = hi_ + dpp<CTRL>(hi_); \
    x[i] = (lane & (BIT)) ? t2_ : t1_;                                 \
  }
  TR_STEP(8, XOR8, 8)
  TR_STEP(4, XOR7, 4)
  TR_STEP(2, XOR2, 2)
  TR_STEP(1, XOR1, 1)
#undef TR_STEP
  float s;
  {
    auto r = __builtin_amdgcn_permlane16_swap(__float_as_uint(x[0]), __float_as_uint(x[0]), false, false);
    s = (__uint_as_float(r[0]) + __uint_as_float(r[1])) * C2;
  }
  float pm = fmaxf(s, dpp<XOR2>(s));
  pm = fmaxf(pm, dpp<ROR4>(pm));
  pm = fmaxf(pm, dpp<XOR8>(pm));
  const float mn = fmaxf(st.m, pm);
  const float alpha = __builtin_amdgcn_exp2f(st.m - mn);
  const float p = __builtin_amdgcn_exp2f(s - mn);
  st.l = st.l * alpha + p;
  st.m = mn;
  const float a0 = dpp<0x00>(alpha), a1 = dpp<0x55>(alpha);
#pragma unroll
  for (int k = 0; k < 8; ++k) { st.acc[k] *= a0; st.acc[8 + k] *= a1; }
#define PV(T)                                                                     \
  {                                                                               \
    const float p0 = dpp<BCAST + 2 * (T)>(p), p1 = dpp<BCAST + 2 * (T) + 1>(p);   \
    _Pragma("unroll") for (int k = 0; k < 4; ++k) {                               \
      const float lo = __uint_as_float(vr[T][k] << 16);                           \
      const float hi = __uint_as_float(vr[T][k] & 0xffff0000u);                   \
      st.acc[2 * k] = fmaf(p0, lo, st.acc[2 * k]);                                \
      st.acc[2 * k + 1] = fmaf(p0, hi, st.acc[2 * k + 1]);                        \
      st.acc[8 + 2 * k] = fmaf(p1, lo, st.acc[8 + 2 * k]);                        \
      st.acc[8 + 2 * k + 1] = fmaf(p1, hi, st.acc[8 + 2 * k + 1]);                \
    }                                                                             \
  }
  PV(0) PV(1) PV(2) PV(3) PV(4) PV(5) PV(6) PV(7)
#undef PV
  if (ISSUE) {
    __builtin_amdgcn_sched_barrier(0);
#pragma unroll
    for (int t = 0; t < 8; ++t) vr[t] = ldnt(vn + t * 1024);
    __builtin_amdgcn_sched_barrier(0);
  }
}

constexpr int REC = 2 * D + 4;

__global__ void __launch_bounds__(NTHR, (NWAVES * (NSPLIT * NGROUP / 256) + 3) / 4)
gqa_decode_split(const unsigned char* __restrict__ qp, const unsigned char* __restrict__ kp,
                 const unsigned char* __restrict__ vp, float* __restrict__ pacc, float* __restrict__ pml) {
  __shared__ __attribute__((aligned(16))) float lds[NWAVES * REC];
  const int tid = threadIdx.x, lane = tid & 63;
  const int wave = __builtin_amdgcn_readfirstlane(tid >> 6);
  const int grp = GROUP_MAJOR ? blockIdx.x / NSPLIT : blockIdx.x % NGROUP;
  const int split = GROUP_MAJOR ? blockIdx.x % NSPLIT : blockIdx.x / NGROUP;

  u32x4 q[2];
  q[0] = *reinterpret_cast<const u32x4*>(qp + (size_t)(grp * 2) * (D * 2) + (lane & 31) * 16);
  q[1] = *reinterpret_cast<const u32x4*>(qp + (size_t)(grp * 2 + 1) * (D * 2) + (lane & 31) * 16);

  constexpr size_t TILE_B = (size_t)TK * D * 2;
  constexpr size_t STRIDE = INTERLEAVE ? TILE_B * NWAVES * NSPLIT : TILE_B * NWAVES;
  const size_t base = (size_t)grp * SK * D * 2 +
                      (INTERLEAVE ? (size_t)split * NWAVES * TILE_B : (size_t)split * NT * NWAVES * TILE_B) +
                      (size_t)wave * TILE_B + lane * 16;
  const unsigned char* kb = kp + base;
  const unsigned char* vb = vp + base;

  u32x4 ka[8], va[8], kb2[8], vb2[8];
#pragma unroll
  for (int t = 0; t < 8; ++t) ka[t] = ldnt(kb + t * 1024);
#pragma unroll
  for (int t = 0; t < 8; ++t) va[t] = ldnt(vb + t * 1024);
#pragma unroll
  for (int t = 0; t < 8; ++t) kb2[t] = ldnt(kb + STRIDE + t * 1024);
#pragma unroll
  for (int t = 0; t < 8; ++t) vb2[t] = ldnt(vb + STRIDE + t * 1024);
  __builtin_amdgcn_sched_barrier(0);

  State st;
  st.m = -INFINITY;
  st.l = 0.f;
#pragma unroll
  for (int k = 0; k < 16; ++k) st.acc[k] = 0.f;

  for (int i = 0; i < NT - 2; i += 2) {
    step<true>(st, ka, va, q, lane, kb + (size_t)(i + 2) * STRIDE, vb + (size_t)(i + 2) * STRIDE);
    step<true>(st, kb2, vb2, q, lane, kb + (size_t)(i + 3) * STRIDE, vb + (size_t)(i + 3) * STRIDE);
  }
  step<false>(st, ka, va, q, lane, nullptr, nullptr);
  step<false>(st, kb2, vb2, q, lane, nullptr, nullptr);

  float l = st.l;
  l += dpp<XOR2>(l);
  l += dpp<ROR4>(l);
  l += dpp<XOR8>(l);
  float m = st.m;
  float mo;
  {
    auto r = __builtin_amdgcn_permlane32_swap(__float_as_uint(m), __float_as_uint(m), false, false);
    mo = fmaxf(__uint_as_float(r[0]), __uint_as_float(r[1]));
  }
  const float w = __builtin_amdgcn_exp2f(m - mo);
  l *= w;
  {
    auto r = __builtin_amdgcn_permlane32_swap(__float_as_uint(l), __float_as_uint(l), false, false);
    l = __uint_as_float(r[0]) + __uint_as_float(r[1]);
  }
  const float w0 = dpp<0x00>(w), w1 = dpp<0x55>(w);
  float o[8];
#pragma unroll
  for (int k = 0; k < 8; ++k) {
    auto r = __builtin_amdgcn_permlane32_swap(__float_as_uint(st.acc[k] * w0), __float_as_uint(st.acc[8 + k] * w1), false, false);
    o[k] = __uint_as_float(r[0]) + __uint_as_float(r[1]);
  }
  float* rec = lds + wave * REC;
  *reinterpret_cast<f32x4*>(rec + lane * 8) = f32x4{o[0], o[1], o[2], o[3]};
  *reinterpret_cast<f32x4*>(rec + lane * 8 + 4) = f32x4{o[4], o[5], o[6], o[7]};
  if (lane < 2) { rec[2 * D + lane] = mo; rec[2 * D + 2 + lane] = l; }
  __syncthreads();

  for (int e = tid; e < 2 * D; e += NTHR) {
    const int g = e >> 8;
    float M = -INFINITY;
#pragma unroll
    for (int j = 0; j < NWAVES; ++j) M = fmaxf(M, lds[j * REC + 2 * D + g]);
    float L = 0.f, acc = 0.f;
#pragma unroll
    for (int j = 0; j < NWAVES; ++j) {
      const float wj = __builtin_amdgcn_exp2f(lds[j * REC + 2 * D + g] - M);
      L = fmaf(wj, lds[j * REC + 2 * D + 2 + g], L);
      acc = fmaf(wj, lds[j * REC + e], acc);
    }
    const size_t slot = (size_t)(grp * 2 + g) * NSPLIT + split;
    pacc[slot * D + (e & 255)] = acc;
    if ((e & 255) == 0) { pml[slot * 2] = M; pml[slot * 2 + 1] = L; }
  }
}

__global__ void __launch_bounds__(256)
gqa_decode_combine(const float* __restrict__ pacc, const float* __restrict__ pml, __hip_bfloat16* __restrict__ out) {
  const int row = blockIdx.x, d = threadIdx.x;
  float M = -INFINITY;
#pragma unroll
  for (int s = 0; s < NSPLIT; ++s) M = fmaxf(M, pml[(row * NSPLIT + s) * 2]);
  float L = 0.f, acc = 0.f;
#pragma unroll
  for (int s = 0; s < NSPLIT; ++s) {
    const float w = __builtin_amdgcn_exp2f(pml[(row * NSPLIT + s) * 2] - M);
    L = fmaf(w, pml[(row * NSPLIT + s) * 2 + 1], L);
    acc = fmaf(w, pacc[((size_t)row * NSPLIT + s) * D + d], acc);
  }
  out[row * D + d] = __float2bfloat16(acc / L);
}

}

extern "C" void kernel_launch(void* const* d_in, const int* in_sizes, int n_in, void* d_out, int out_size, void* d_ws,
                              size_t ws_size, hipStream_t stream) {
  const unsigned char* q = (const unsigned char*)d_in[0];
  const unsigned char* k = (const unsigned char*)d_in[1];
  const unsigned char* v = (const unsigned char*)d_in[2];
  float* pml = (float*)d_ws;
  float* pacc = (float*)((char*)d_ws + 65536);
  gqa_decode_split<<<NGROUP * NSPLIT, NTHR, 0, stream>>>(q, k, v, pacc, pml);
  gqa_decode_combine<<<B * HQ, 256, 0, stream>>>(pacc, pml, (__hip_bfloat16*)d_out);
}
```

```cpp
#define TL 8
#define RS 1
#include <hip/hip_runtime.h>
#include <hip/hip_bf16.h>
#include <stdint.h>

namespace {

constexpr int B = 4, HQ = 16, HKV = 8, G = HQ / HKV, SK = 16384, D = 256;
constexpr int NGROUP = B * HKV;
constexpr int NSPLIT = 8, NWAVES = 4, NTHR = NWAVES * 64;
constexpr int F = TL * RS;
constexpr int NLOAD = SK / (NSPLIT * NWAVES * 2);
constexpr int NI = NLOAD / F;
constexpr unsigned ROWB = NWAVES * TL * 1024;
static_assert(G == 2, "layout assumes 2 query heads per kv head");
static_assert(F % 8 == 0 && TL % 2 == 0 && NLOAD % F == 0, "ring shape");

typedef unsigned u32x4 __attribute__((ext_vector_type(4)));
typedef float f32x4 __attribute__((ext_vector_type(4)));
typedef float f32x2 __attribute__((ext_vector_type(2)));
typedef __bf16 bf16x2 __attribute__((ext_vector_type(2)));

__device__ __forceinline__ float dot2(unsigned a, unsigned b, float acc) {
  return __builtin_amdgcn_fdot2_f32_bf16(__builtin_bit_cast(bf16x2, a), __builtin_bit_cast(bf16x2, b), acc, false);
}
template <int CTRL> __device__ __forceinline__ float dpp(float x) {
  return __builtin_bit_cast(float, __builtin_amdgcn_mov_dpp(__builtin_bit_cast(int, x), CTRL, 0xf, 0xf, true));
}
constexpr int XOR1 = 0xB1, XOR2 = 0x4E, XOR7 = 0x141, XOR8 = 0x128, ROR4 = 0x124;

template <int OFF> __device__ __forceinline__ void ldnt(u32x4& d, unsigned voff, const unsigned char* sb) {
  asm volatile("global_load_dwordx4 %0, %1, %2 offset:%3 nt" : "=v"(d) : "v"(voff), "s"(sb), "i"(OFF) : "memory");
}
__device__ __forceinline__ float swap16_sum(float x) {
  auto r = __builtin_amdgcn_permlane16_swap(__float_as_uint(x), __float_as_uint(x), false, false);
  return __uint_as_float(r[0]) + __uint_as_float(r[1]);
}
__device__ __forceinline__ float swap32_sum(float x) {
  auto r = __builtin_amdgcn_permlane32_swap(__float_as_uint(x), __float_as_uint(x), false, false);
  return __uint_as_float(r[0]) + __uint_as_float(r[1]);
}
__device__ __forceinline__ float wave_sum(float x) {
  x += dpp<XOR1>(x); x += dpp<XOR2>(x); x += dpp<ROR4>(x); x += dpp<XOR8>(x);
  return swap32_sum(swap16_sum(x));
}

__device__ __forceinline__ void issue(u32x4 (&r)[TL], const unsigned char* base, unsigned voff) {
  if constexpr (TL == 8) {
    ldnt<-4096>(r[0], voff, base); ldnt<-3072>(r[1], voff, base); ldnt<-2048>(r[2], voff, base); ldnt<-1024>(r[3], voff, base);
    ldnt<0>(r[4], voff, base); ldnt<1024>(r[5], voff, base); ldnt<2048>(r[6], voff, base); ldnt<3072>(r[7], voff, base);
  } else if constexpr (TL == 4) {
    ldnt<0>(r[0], voff, base); ldnt<1024>(r[1], voff, base); ldnt<2048>(r[2], voff, base); ldnt<3072>(r[3], voff, base);
  } else {
    ldnt<0>(r[0], voff, base); ldnt<1024>(r[1], voff, base);
  }
}
template <int N> __device__ __forceinline__ void arrive(u32x4 (&r)[TL]) {
  if constexpr (TL == 8)
    asm volatile("s_waitcnt vmcnt(%8)" : "+v"(r[0]), "+v"(r[1]), "+v"(r[2]), "+v"(r[3]), "+v"(r[4]), "+v"(r[5]), "+v"(r[6]), "+v"(r[7]) : "i"(N) : "memory");
  else if constexpr (TL == 4)
    asm volatile("s_waitcnt vmcnt(%4)" : "+v"(r[0]), "+v"(r[1]), "+v"(r[2]), "+v"(r[3]) : "i"(N) : "memory");
  else
    asm volatile("s_waitcnt vmcnt(%2)" : "+v"(r[0]), "+v"(r[1]) : "i"(N) : "memory");
}

template <int N> struct IC { static constexpr int value = N; };
template <int N, int I = 0, class Fn> __device__ __forceinline__ void static_for(Fn&& fn) {
  if constexpr (I < N) { fn(IC<I>{}); static_for<N, I + 1>(fn); }
}

constexpr int REC = 2 * D + 4;
constexpr int WLDS = 1024;

__global__ void __launch_bounds__(NTHR, 1)
gqa_decode_split(const unsigned char* __restrict__ qp, const unsigned char* __restrict__ kp,
                 const unsigned char* __restrict__ vp, float* __restrict__ pacc, float* __restrict__ pml) {
  __shared__ __attribute__((aligned(16))) float lds[NWAVES * WLDS];
  const int tid = threadIdx.x, lane = tid & 63;
  const int wave = __builtin_amdgcn_readfirstlane(tid >> 6);
  const int grp = blockIdx.x / NSPLIT, split = blockIdx.x % NSPLIT;
  constexpr float C2 = 0.0625f * 1.4426950408889634f;

  const size_t base = (size_t)blockIdx.x * (SK / NSPLIT) * D * 2 + (size_t)wave * TL * 1024 + (TL == 8 ? 4096 : 0);
  const unsigned char* kb = kp + base;
  const unsigned char* vb = vp + base;
  const unsigned voff = lane * 16;

  u32x4 q[2];
  {
    const unsigned char* qb = qp + (size_t)grp * (2 * D * 2);
    const unsigned qoff = (lane & 31) * 16;
    asm volatile("global_load_dwordx4 %0, %1, %2" : "=v"(q[0]) : "v"(qoff), "s"(qb) : "memory");
    asm volatile("global_load_dwordx4 %0, %1, %2 offset:512" : "=v"(q[1]) : "v"(qoff), "s"(qb) : "memory");
  }
  u32x4 r[RS][TL];
#pragma unroll
  for (int s = 0; s < RS; ++s) issue(r[s], kb + s * ROWB, voff);
  asm volatile("s_waitcnt vmcnt(%2)" : "+v"(q[0]), "+v"(q[1]) : "i"(F) : "memory");

  float* sc = lds + wave * WLDS;
  const int h = lane >> 5;

  float mx = -INFINITY;
  for (int it = 0; it < NI; ++it) {
    const unsigned char* nxt = (it + 1 < NI) ? kb + (size_t)(it + 1) * RS * ROWB : vb;
    float x[16];
#pragma unroll
    for (int s = 0; s < RS; ++s) {
      arrive<F - TL>(r[s]);
#pragma unroll
      for (int j = 0; j < TL; ++j)
#pragma unroll
        for (int g = 0; g < 2; ++g) {
          float d = 0.f;
#pragma unroll
          for (int k = 0; k < 4; ++k) d = dot2(r[s][j][k], q[g][k], d);
          x[((s * TL + j) & 7) * 2 + g] = d;
        }
#pragma unroll
      for (int j = 0; j < TL; ++j) asm volatile("" : "+v"(x[((s * TL + j) & 7) * 2]), "+v"(x[((s * TL + j) & 7) * 2 + 1]));
      issue(r[s], nxt + s * ROWB, voff);
      if ((((s + 1) * TL) & 7) == 0) {
#define TR_STEP(HALF, CTRL, BIT)                                        \
  _Pragma("unroll") for (int i = 0; i < (HALF); ++i) {                  \
    const float lo_ = x[i], hi_ = x[i + (HALF)];                        \
    const float t1_ = lo_ + dpp<CTRL>(lo_), t2_ = hi_ + dpp<CTRL>(hi_); \
    x[i] = (lane & (BIT)) ? t2_ : t1_;                                  \
  }
        TR_STEP(8, XOR8, 8)
        TR_STEP(4, XOR7, 4)
        TR_STEP(2, XOR2, 2)
        TR_STEP(1, XOR1, 1)
#undef TR_STEP
        const float sv = swap16_sum(x[0]);
        mx = fmaxf(mx, sv);
        if ((lane & 16) == 0) sc[(it * (F / 8) + (s * TL) / 8) * 32 + h * 16 + (lane & 15)] = sv;
      }
    }
  }

  mx = fmaxf(mx, dpp<XOR2>(mx));
  mx = fmaxf(mx, dpp<ROR4>(mx));
  mx = fmaxf(mx, dpp<XOR8>(mx));
  {
    auto t = __builtin_amdgcn_permlane32_swap(__float_as_uint(mx), __float_as_uint(mx), false, false);
    mx = fmaxf(__uint_as_float(t[0]), __uint_as_float(t[1]));
  }
  const float m0 = __int_as_float(__builtin_amdgcn_readlane(__float_as_int(mx), 0)) * C2;
  const float m1 = __int_as_float(__builtin_amdgcn_readlane(__float_as_int(mx), 1)) * C2;
  float l0 = 0.f, l1 = 0.f;
#pragma unroll
  for (int i = 0; i < 4; ++i) {
    f32x4 t = *reinterpret_cast<f32x4*>(sc + i * 256 + lane * 4);
    t[0] = __builtin_amdgcn_exp2f(fmaf(t[0], C2, -m0));
    t[1] = __builtin_amdgcn_exp2f(fmaf(t[1], C2, -m1));
    t[2] = __builtin_amdgcn_exp2f(fmaf(t[2], C2, -m0));
    t[3] = __builtin_amdgcn_exp2f(fmaf(t[3], C2, -m1));
    l0 += t[0] + t[2];
    l1 += t[1] + t[3];
    *reinterpret_cast<f32x4*>(sc + i * 256 + lane * 4) = t;
  }
  l0 = wave_sum(l0);
  l1 = wave_sum(l1);

  f32x2 a0[4], a1[4];
#pragma unroll
  for (int k = 0; k < 4; ++k) { a0[k] = f32x2{0.f, 0.f}; a1[k] = f32x2{0.f, 0.f}; }
  auto pv = [&](auto nwait, int it, int s) {
    const float* pp = sc + (it * (F / 8) + (s * TL) / 8) * 32 + h * 16 + ((s * TL) & 7) * 2;
    f32x4 p[TL / 2];
#pragma unroll
    for (int j = 0; j < TL / 2; ++j) p[j] = *reinterpret_cast<const f32x4*>(pp + j * 4);
    arrive<decltype(nwait)::value>(r[s]);
#pragma unroll
    for (int j = 0; j < TL; ++j)
#pragma unroll
      for (int k = 0; k < 4; ++k) {
        const unsigned w = r[s][j][k];
        const f32x2 v = f32x2{__uint_as_float(w << 16), __uint_as_float(w & 0xffff0000u)};
        const float p0 = p[j >> 1][(j & 1) * 2], p1 = p[j >> 1][(j & 1) * 2 + 1];
        a0[k] = __builtin_elementwise_fma(v, f32x2{p0, p0}, a0[k]);
        a1[k] = __builtin_elementwise_fma(v, f32x2{p1, p1}, a1[k]);
      }
#pragma unroll
    for (int k = 0; k < 4; ++k) asm volatile("" : "+v"(a0[k]), "+v"(a1[k]));
  };
  for (int it = 0; it < NI - 1; ++it) {
    const unsigned char* nxt = vb + (size_t)(it + 1) * RS * ROWB;
#pragma unroll
    for (int s = 0; s < RS; ++s) {
      pv(IC<F - TL>{}, it, s);
      issue(r[s], nxt + s * ROWB, voff);
    }
  }
  static_for<RS>([&](auto s) { pv(IC<F - (decltype(s)::value + 1) * TL>{}, NI - 1, decltype(s)::value); });

  float o[8];
#pragma unroll
  for (int k = 0; k < 8; ++k) {
    auto t = __builtin_amdgcn_permlane32_swap(__float_as_uint(a0[k >> 1][k & 1]), __float_as_uint(a1[k >> 1][k & 1]), false, false);
    o[k] = __uint_as_float(t[0]) + __uint_as_float(t[1]);
  }
  *reinterpret_cast<f32x4*>(sc + lane * 8) = f32x4{o[0], o[1], o[2], o[3]};
  *reinterpret_cast<f32x4*>(sc + lane * 8 + 4) = f32x4{o[4], o[5], o[6], o[7]};
  if (lane == 0) *reinterpret_cast<f32x4*>(sc + 2 * D) = f32x4{m0, m1, l0, l1};
  __syncthreads();

#pragma unroll
  for (int g = 0; g < 2; ++g) {
    float M = -INFINITY;
#pragma unroll
    for (int j = 0; j < NWAVES; ++j) M = fmaxf(M, lds[j * WLDS + 2 * D + g]);
    float L = 0.f, acc = 0.f;
#pragma unroll
    for (int j = 0; j < NWAVES; ++j) {
      const float wj = __builtin_amdgcn_exp2f(lds[j * WLDS + 2 * D + g] - M);
      L = fmaf(wj, lds[j * WLDS + 2 * D + 2 + g], L);
      acc = fmaf(wj, lds[j * WLDS + g * D + tid], acc);
    }
    const size_t slot = (size_t)(grp * 2 + g) * NSPLIT + split;
    pacc[slot * D + tid] = acc;
    if (tid == 0) { pml[slot * 2] = M; pml[slot * 2 + 1] = L; }
  }
}

__global__ void __launch_bounds__(256)
gqa_decode_combine(const float* __restrict__ pacc, const float* __restrict__ pml, __hip_bfloat16* __restrict__ out) {
  const int row = blockIdx.x, d = threadIdx.x;
  float M = -INFINITY;
#pragma unroll
  for (int s = 0; s < NSPLIT; ++s) M = fmaxf(M, pml[(row * NSPLIT + s) * 2]);
  float L = 0.f, acc = 0.f;
#pragma unroll
  for (int s = 0; s < NSPLIT; ++s) {
    const float w = __builtin_amdgcn_exp2f(pml[(row * NSPLIT + s) * 2] - M);
    L = fmaf(w, pml[(row * NSPLIT + s) * 2 + 1], L);
    acc = fmaf(w, pacc[((size_t)row * NSPLIT + s) * D + d], acc);
  }
  out[row * D + d] = __float2bfloat16(acc / L);
}

}

extern "C" void kernel_launch(void* const* d_in, const int* in_sizes, int n_in, void* d_out, int out_size, void* d_ws,
                              size_t ws_size, hipStream_t stream) {
  const unsigned char* q = (const unsigned char*)d_in[0];
  const unsigned char* k = (const unsigned char*)d_in[1];
  const unsigned char* v = (const unsigned char*)d_in[2];
  float* pml = (float*)d_ws;
  float* pacc = (float*)((char*)d_ws + 65536);
  gqa_decode_split<<<NGROUP * NSPLIT, NTHR, 0, stream>>>(q, k, v, pacc, pml);
  gqa_decode_combine<<<B * HQ, 256, 0, stream>>>(pacc, pml, (__hip_bfloat16*)d_out);
}
```

```cpp
#define TL 8
#define RS 1
#define VDEF_FROM 112
#include <hip/hip_runtime.h>
#include <hip/hip_bf16.h>
#include <stdint.h>

#define STAMP(ptr, i) do {} while (0)

namespace {

constexpr int B = 4, HQ = 16, HKV = 8, G = HQ / HKV, SK = 16384, D = 256;
constexpr int NGROUP = B * HKV;
constexpr int NSPLIT = 8, NWAVES = 4, NTHR = NWAVES * 64;
constexpr int F = TL * RS;
constexpr int NLOAD = SK / (NSPLIT * NWAVES * 2);
constexpr int NI = NLOAD / F;
constexpr unsigned ROWB = NWAVES * TL * 1024;
static_assert(G == 2, "layout assumes 2 query heads per kv head");
static_assert(F % 8 == 0 && TL % 2 == 0 && NLOAD % F == 0, "ring shape");

typedef unsigned u32x4 __attribute__((ext_vector_type(4)));
typedef float f32x4 __attribute__((ext_vector_type(4)));
typedef float f32x2 __attribute__((ext_vector_type(2)));
typedef __bf16 bf16x2 __attribute__((ext_vector_type(2)));

__device__ __forceinline__ float dot2(unsigned a, unsigned b, float acc) {
  return __builtin_amdgcn_fdot2_f32_bf16(__builtin_bit_cast(bf16x2, a), __builtin_bit_cast(bf16x2, b), acc, false);
}
template <int CTRL> __device__ __forceinline__ float dpp(float x) {
  return __builtin_bit_cast(float, __builtin_amdgcn_mov_dpp(__builtin_bit_cast(int, x), CTRL, 0xf, 0xf, true));
}
constexpr int XOR1 = 0xB1, XOR2 = 0x4E, XOR7 = 0x141, XOR8 = 0x128, ROR4 = 0x124;

__device__ __forceinline__ float swap16_sum(float x) {
  auto r = __builtin_amdgcn_permlane16_swap(__float_as_uint(x), __float_as_uint(x), false, false);
  return __uint_as_float(r[0]) + __uint_as_float(r[1]);
}
__device__ __forceinline__ float swap32_sum(float x) {
  auto r = __builtin_amdgcn_permlane32_swap(__float_as_uint(x), __float_as_uint(x), false, false);
  return __uint_as_float(r[0]) + __uint_as_float(r[1]);
}
__device__ __forceinline__ float wave_sum(float x) {
  x += dpp<XOR1>(x); x += dpp<XOR2>(x); x += dpp<ROR4>(x); x += dpp<XOR8>(x);
  return swap32_sum(swap16_sum(x));
}

#define LD_(R, OFF, POL) "global_load_dwordx4 %" #R ", %[vo], %[sb] offset:" #OFF POL "\n\t"
#define LD8_(POL) LD_(0, -4096, POL) LD_(1, -3072, POL) LD_(2, -2048, POL) LD_(3, -1024, POL) LD_(4, 0, POL) LD_(5, 1024, POL) LD_(6, 2048, POL) LD_(7, 3072, POL)
#define LD4_(POL) LD_(0, 0, POL) LD_(1, 1024, POL) LD_(2, 2048, POL) LD_(3, 3072, POL)
#define LD2_(POL) LD_(0, 0, POL) LD_(1, 1024, POL)
#define SEL_(LDS_) "s_cmp_eq_u32 %[kp], 0\n\ts_cbranch_scc1 1f\n\t" LDS_("") "s_branch 2f\n1:\n\t" LDS_(" nt") "2:"
__device__ __forceinline__ void issue(u32x4 (&r)[TL], const unsigned char* base, unsigned voff, int keep) {
  if constexpr (TL == 8)
    asm volatile(SEL_(LD8_) : "=&v"(r[0]), "=&v"(r[1]), "=&v"(r[2]), "=&v"(r[3]), "=&v"(r[4]), "=&v"(r[5]), "=&v"(r[6]), "=&v"(r[7])
                 : [vo] "v"(voff), [sb] "s"(base), [kp] "s"(keep) : "memory", "scc");
  else if constexpr (TL == 4)
    asm volatile(SEL_(LD4_) : "=&v"(r[0]), "=&v"(r[1]), "=&v"(r[2]), "=&v"(r[3])
                 : [vo] "v"(voff), [sb] "s"(base), [kp] "s"(keep) : "memory", "scc");
  else
    asm volatile(SEL_(LD2_) : "=&v"(r[0]), "=&v"(r[1])
                 : [vo] "v"(voff), [sb] "s"(base), [kp] "s"(keep) : "memory", "scc");
}
template <int N> __device__ __forceinline__ void arrive(u32x4 (&r)[TL]) {
  if constexpr (TL == 8)
    asm volatile("s_waitcnt vmcnt(%8)" : "+v"(r[0]), "+v"(r[1]), "+v"(r[2]), "+v"(r[3]), "+v"(r[4]), "+v"(r[5]), "+v"(r[6]), "+v"(r[7]) : "i"(N) : "memory");
  else if constexpr (TL == 4)
    asm volatile("s_waitcnt vmcnt(%4)" : "+v"(r[0]), "+v"(r[1]), "+v"(r[2]), "+v"(r[3]) : "i"(N) : "memory");
  else
    asm volatile("s_waitcnt vmcnt(%2)" : "+v"(r[0]), "+v"(r[1]) : "i"(N) : "memory");
}

template <int N> struct IC { static constexpr int value = N; };
template <int N, int I = 0, class Fn> __device__ __forceinline__ void static_for(Fn&& fn) {
  if constexpr (I < N) { fn(IC<I>{}); static_for<N, I + 1>(fn); }
}

constexpr int REC = 2 * D + 4;
constexpr int WLDS = 1024;

__global__ void __launch_bounds__(NTHR, 1)
gqa_decode_split(const unsigned char* __restrict__ qp, const unsigned char* __restrict__ kp,
                 const unsigned char* __restrict__ vp, float* __restrict__ pacc, float* __restrict__ pml) {
  __shared__ __attribute__((aligned(16))) float lds[NWAVES * WLDS];
  const int tid = threadIdx.x, lane = tid & 63;
  const int wave = __builtin_amdgcn_readfirstlane(tid >> 6);
  const int grp = blockIdx.x / NSPLIT, split = blockIdx.x % NSPLIT;
  constexpr float C2 = 0.0625f * 1.4426950408889634f;

  const size_t base = (size_t)blockIdx.x * (SK / NSPLIT) * D * 2 + (size_t)wave * TL * 1024 + (TL == 8 ? 4096 : 0);
  const unsigned char* kb = kp + base;
  const unsigned char* vb = vp + base;
  const unsigned voff = lane * 16;
  const int keepk = 0, keepv = __builtin_amdgcn_readfirstlane(blockIdx.x >= VDEF_FROM ? 1 : 0);

  u32x4 q[2];
  {
    const unsigned char* qb = qp + (size_t)grp * (2 * D * 2);
    const unsigned qoff = (lane & 31) * 16;
    asm volatile("global_load_dwordx4 %0, %1, %2" : "=v"(q[0]) : "v"(qoff), "s"(qb) : "memory");
    asm volatile("global_load_dwordx4 %0, %1, %2 offset:512" : "=v"(q[1]) : "v"(qoff), "s"(qb) : "memory");
  }
  u32x4 r[RS][TL];
#pragma unroll
  for (int s = 0; s < RS; ++s) issue(r[s], kb + s * ROWB, voff, keepk);
  asm volatile("s_waitcnt vmcnt(%2)" : "+v"(q[0]), "+v"(q[1]) : "i"(F) : "memory");

  float* sc = lds + wave * WLDS;
  const int h = lane >> 5;

  float mx = -INFINITY;
  for (int it = 0; it < NI; ++it) {
    const unsigned char* nxt = (it + 1 < NI) ? kb + (size_t)(it + 1) * RS * ROWB : vb;
    const int keepn = (it + 1 < NI) ? keepk : keepv;
    float x[16];
#pragma unroll
    for (int s = 0; s < RS; ++s) {
      arrive<F - TL>(r[s]);
#pragma unroll
      for (int j = 0; j < TL; ++j)
#pragma unroll
        for (int g = 0; g < 2; ++g) {
          float d = 0.f;
#pragma unroll
          for (int k = 0; k < 4; ++k) d = dot2(r[s][j][k], q[g][k], d);
          x[((s * TL + j) & 7) * 2 + g] = d;
        }
#pragma unroll
      for (int j = 0; j < TL; ++j) asm volatile("" : "+v"(x[((s * TL + j) & 7) * 2]), "+v"(x[((s * TL + j) & 7) * 2 + 1]));
      issue(r[s], nxt + s * ROWB, voff, keepn);
      if ((((s + 1) * TL) & 7) == 0) {
#define TR_STEP(HALF, CTRL, BIT)                                        \
  _Pragma("unroll") for (int i = 0; i < (HALF); ++i) {                  \
    const float lo_ = x[i], hi_ = x[i + (HALF)];                        \
    const float t1_ = lo_ + dpp<CTRL>(lo_), t2_ = hi_ + dpp<CTRL>(hi_); \
    x[i] = (lane & (BIT)) ? t2_ : t1_;                                  \
  }
        TR_STEP(8, XOR8, 8)
        TR_STEP(4, XOR7, 4)
        TR_STEP(2, XOR2, 2)
        TR_STEP(1, XOR1, 1)
#undef TR_STEP
        const float sv = swap16_sum(x[0]);
        mx = fmaxf(mx, sv);
        if ((lane & 16) == 0) sc[(it * (F / 8) + (s * TL) / 8) * 32 + h * 16 + (lane & 15)] = sv;
      }
    }
  }

  mx = fmaxf(mx, dpp<XOR2>(mx));
  mx = fmaxf(mx, dpp<ROR4>(mx));
  mx = fmaxf(mx, dpp<XOR8>(mx));
  {
    auto t = __builtin_amdgcn_permlane32_swap(__float_as_uint(mx), __float_as_uint(mx), false, false);
    mx = fmaxf(__uint_as_float(t[0]), __uint_as_float(t[1]));
  }
  const float m0 = __int_as_float(__builtin_amdgcn_readlane(__float_as_int(mx), 0)) * C2;
  const float m1 = __int_as_float(__builtin_amdgcn_readlane(__float_as_int(mx), 1)) * C2;
  float l0 = 0.f, l1 = 0.f;
#pragma unroll
  for (int i = 0; i < 4; ++i) {
    f32x4 t = *reinterpret_cast<f32x4*>(sc + i * 256 + lane * 4);
    t[0] = __builtin_amdgcn_exp2f(fmaf(t[0], C2, -m0));
    t[1] = __builtin_amdgcn_exp2f(fmaf(t[1], C2, -m1));
    t[2] = __builtin_amdgcn_exp2f(fmaf(t[2], C2, -m0));
    t[3] = __builtin_amdgcn_exp2f(fmaf(t[3], C2, -m1));
    l0 += t[0] + t[2];
    l1 += t[1] + t[3];
    *reinterpret_cast<f32x4*>(sc + i * 256 + lane * 4) = t;
  }
  l0 = wave_sum(l0);
  l1 = wave_sum(l1);

  f32x2 a0[4], a1[4];
#pragma unroll
  for (int k = 0; k < 4; ++k) { a0[k] = f32x2{0.f, 0.f}; a1[k] = f32x2{0.f, 0.f}; }
  auto pv = [&](auto nwait, int it, int s) {
    const float* pp = sc + (it * (F / 8) + (s * TL) / 8) * 32 + h * 16 + ((s * TL) & 7) * 2;
    f32x4 p[TL / 2];
#pragma unroll
    for (int j = 0; j < TL / 2; ++j) p[j] = *reinterpret_cast<const f32x4*>(pp + j * 4);
    arrive<decltype(nwait)::value>(r[s]);
#pragma unroll
    for (int j = 0; j < TL; ++j)
#pragma unroll
      for (int k = 0; k < 4; ++k) {
        const unsigned w = r[s][j][k];
        const f32x2 v = f32x2{__uint_as_float(w << 16), __uint_as_float(w & 0xffff0000u)};
        const float p0 = p[j >> 1][(j & 1) * 2], p1 = p[j >> 1][(j & 1) * 2 + 1];
        a0[k] = __builtin_elementwise_fma(v, f32x2{p0, p0}, a0[k]);
        a1[k] = __builtin_elementwise_fma(v, f32x2{p1, p1}, a1[k]);
      }
#pragma unroll
    for (int k = 0; k < 4; ++k) asm volatile("" : "+v"(a0[k]), "+v"(a1[k]));
  };
  for (int it = 0; it < NI - 1; ++it) {
    const unsigned char* nxt = vb + (size_t)(it + 1) * RS * ROWB;
#pragma unroll
    for (int s = 0; s < RS; ++s) {
      pv(IC<F - TL>{}, it, s);
      issue(r[s], nxt + s * ROWB, voff, keepv);
    }
  }
  static_for<RS>([&](auto s) { pv(IC<F - (decltype(s)::value + 1) * TL>{}, NI - 1, decltype(s)::value); });

  float o[8];
#pragma unroll
  for (int k = 0; k < 8; ++k) {
    auto t = __builtin_amdgcn_permlane32_swap(__float_as_uint(a0[k >> 1][k & 1]), __float_as_uint(a1[k >> 1][k & 1]), false, false);
    o[k] = __uint_as_float(t[0]) + __uint_as_float(t[1]);
  }
  *reinterpret_cast<f32x4*>(sc + lane * 8) = f32x4{o[0], o[1], o[2], o[3]};
  *reinterpret_cast<f32x4*>(sc + lane * 8 + 4) = f32x4{o[4], o[5], o[6], o[7]};
  if (lane == 0) *reinterpret_cast<f32x4*>(sc + 2 * D) = f32x4{m0, m1, l0, l1};
  __syncthreads();

#pragma unroll
  for (int g = 0; g < 2; ++g) {
    float M = -INFINITY;
#pragma unroll
    for (int j = 0; j < NWAVES; ++j) M = fmaxf(M, lds[j * WLDS + 2 * D + g]);
    float L = 0.f, acc = 0.f;
#pragma unroll
    for (int j = 0; j < NWAVES; ++j) {
      const float wj = __builtin_amdgcn_exp2f(lds[j * WLDS + 2 * D + g] - M);
      L = fmaf(wj, lds[j * WLDS + 2 * D + 2 + g], L);
      acc = fmaf(wj, lds[j * WLDS + g * D + tid], acc);
    }
    const size_t slot = (size_t)(grp * 2 + g) * NSPLIT + split;
    pacc[slot * D + tid] = acc;
    if (tid == 0) { pml[slot * 2] = M; pml[slot * 2 + 1] = L; }
  }
}

__global__ void __launch_bounds__(256)
gqa_decode_combine(const float* __restrict__ pacc, const float* __restrict__ pml, __hip_bfloat16* __restrict__ out) {
  const int row = blockIdx.x, d = threadIdx.x;
  float M = -INFINITY;
#pragma unroll
  for (int s = 0; s < NSPLIT; ++s) M = fmaxf(M, pml[(row * NSPLIT + s) * 2]);
  float L = 0.f, acc = 0.f;
#pragma unroll
  for (int s = 0; s < NSPLIT; ++s) {
    const float w = __builtin_amdgcn_exp2f(pml[(row * NSPLIT + s) * 2] - M);
    L = fmaf(w, pml[(row * NSPLIT + s) * 2 + 1], L);
    acc = fmaf(w, pacc[((size_t)row * NSPLIT + s) * D + d], acc);
  }
  out[row * D + d] = __float2bfloat16(acc / L);
}

}

extern "C" void kernel_launch(void* const* d_in, const int* in_sizes, int n_in, void* d_out, int out_size, void* d_ws,
                              size_t ws_size, hipStream_t stream) {
  const unsigned char* q = (const unsigned char*)d_in[0];
  const unsigned char* k = (const unsigned char*)d_in[1];
  const unsigned char* v = (const unsigned char*)d_in[2];
  float* pml = (float*)d_ws;
  float* pacc = (float*)((char*)d_ws + 65536);
  gqa_decode_split<<<NGROUP * NSPLIT, NTHR, 0, stream>>>(q, k, v, pacc, pml);
  gqa_decode_combine<<<B * HQ, 256, 0, stream>>>(pacc, pml, (__hip_bfloat16*)d_out);
}
```

```cpp
#define TL 8
#define RS 1
#define VDEF_FROM 256
#define STAMPS
#define STAMP_OFF 4194304
#define DIAG 0
#include <hip/hip_runtime.h>
#include <hip/hip_bf16.h>
#include <stdint.h>

#define STAMP(ptr, i) do { if (threadIdx.x == 0) { ((unsigned long long*)((char*)(ptr) + STAMP_OFF))[i] = __builtin_amdgcn_s_memrealtime(); } } while (0)

namespace {

constexpr int B = 4, HQ = 16, HKV = 8, G = HQ / HKV, SK = 16384, D = 256;
constexpr int NGROUP = B * HKV;
constexpr int NSPLIT = 8, NWAVES = 4, NTHR = NWAVES * 64;
constexpr int F = TL * RS;
constexpr int NLOAD = SK / (NSPLIT * NWAVES * 2);
constexpr int NI = NLOAD / F;
constexpr unsigned ROWB = NWAVES * TL * 1024;
static_assert(G == 2, "layout assumes 2 query heads per kv head");
static_assert(F % 8 == 0 && TL % 2 == 0 && NLOAD % F == 0, "ring shape");

typedef unsigned u32x4 __attribute__((ext_vector_type(4)));
typedef float f32x4 __attribute__((ext_vector_type(4)));
typedef float f32x2 __attribute__((ext_vector_type(2)));
typedef __bf16 bf16x2 __attribute__((ext_vector_type(2)));

__device__ __forceinline__ float dot2(unsigned a, unsigned b, float acc) {
  return __builtin_amdgcn_fdot2_f32_bf16(__builtin_bit_cast(bf16x2, a), __builtin_bit_cast(bf16x2, b), acc, false);
}
template <int CTRL> __device__ __forceinline__ float dpp(float x) {
  return __builtin_bit_cast(float, __builtin_amdgcn_mov_dpp(__builtin_bit_cast(int, x), CTRL, 0xf, 0xf, true));
}
constexpr int XOR1 = 0xB1, XOR2 = 0x4E, XOR7 = 0x141, XOR8 = 0x128, ROR4 = 0x124;

__device__ __forceinline__ float swap16_sum(float x) {
  auto r = __builtin_amdgcn_permlane16_swap(__float_as_uint(x), __float_as_uint(x), false, false);
  return __uint_as_float(r[0]) + __uint_as_float(r[1]);
}
__device__ __forceinline__ float swap32_sum(float x) {
  auto r = __builtin_amdgcn_permlane32_swap(__float_as_uint(x), __float_as_uint(x), false, false);
  return __uint_as_float(r[0]) + __uint_as_float(r[1]);
}
__device__ __forceinline__ float wave_sum(float x) {
  x += dpp<XOR1>(x); x += dpp<XOR2>(x); x += dpp<ROR4>(x); x += dpp<XOR8>(x);
  return swap32_sum(swap16_sum(x));
}

#define LD_(R, OFF, POL) "global_load_dwordx4 %" #R ", %[vo], %[sb] offset:" #OFF POL "\n\t"
#define LD8_(POL) LD_(0, -4096, POL) LD_(1, -3072, POL) LD_(2, -2048, POL) LD_(3, -1024, POL) LD_(4, 0, POL) LD_(5, 1024, POL) LD_(6, 2048, POL) LD_(7, 3072, POL)
#define LD4_(POL) LD_(0, 0, POL) LD_(1, 1024, POL) LD_(2, 2048, POL) LD_(3, 3072, POL)
#define LD2_(POL) LD_(0, 0, POL) LD_(1, 1024, POL)
#define SEL_(LDS_) "s_cmp_eq_u32 %[kp], 0\n\ts_cbranch_scc1 1f\n\t" LDS_("") "s_branch 2f\n1:\n\t" LDS_(" nt") "2:"
__device__ __forceinline__ void issue(u32x4 (&r)[TL], const unsigned char* base, unsigned voff, int keep) {
  if constexpr (TL == 8)
    asm volatile(SEL_(LD8_) : "=&v"(r[0]), "=&v"(r[1]), "=&v"(r[2]), "=&v"(r[3]), "=&v"(r[4]), "=&v"(r[5]), "=&v"(r[6]), "=&v"(r[7])
                 : [vo] "v"(voff), [sb] "s"(base), [kp] "s"(keep) : "memory", "scc");
  else if constexpr (TL == 4)
    asm volatile(SEL_(LD4_) : "=&v"(r[0]), "=&v"(r[1]), "=&v"(r[2]), "=&v"(r[3])
                 : [vo] "v"(voff), [sb] "s"(base), [kp] "s"(keep) : "memory", "scc");
  else
    asm volatile(SEL_(LD2_) : "=&v"(r[0]), "=&v"(r[1])
                 : [vo] "v"(voff), [sb] "s"(base), [kp] "s"(keep) : "memory", "scc");
}
template <int N> __device__ __forceinline__ void arrive(u32x4 (&r)[TL]) {
  if constexpr (TL == 8)
    asm volatile("s_waitcnt vmcnt(%8)" : "+v"(r[0]), "+v"(r[1]), "+v"(r[2]), "+v"(r[3]), "+v"(r[4]), "+v"(r[5]), "+v"(r[6]), "+v"(r[7]) : "i"(N) : "memory");
  else if constexpr (TL == 4)
    asm volatile("s_waitcnt vmcnt(%4)" : "+v"(r[0]), "+v"(r[1]), "+v"(r[2]), "+v"(r[3]) : "i"(N) : "memory");
  else
    asm volatile("s_waitcnt vmcnt(%2)" : "+v"(r[0]), "+v"(r[1]) : "i"(N) : "memory");
}

template <int N> struct IC { static constexpr int value = N; };
template <int N, int I = 0, class Fn> __device__ __forceinline__ void static_for(Fn&& fn) {
  if constexpr (I < N) { fn(IC<I>{}); static_for<N, I + 1>(fn); }
}

constexpr int REC = 2 * D + 4;
constexpr int WLDS = 1024;

__global__ void __launch_bounds__(NTHR, 1)
gqa_decode_split(const unsigned char* __restrict__ qp, const unsigned char* __restrict__ kp,
                 const unsigned char* __restrict__ vp, float* __restrict__ pacc, float* __restrict__ pml) {
  __shared__ __attribute__((aligned(16))) float lds[NWAVES * WLDS];
  unsigned long long ts0 = __builtin_amdgcn_s_memrealtime();
  const int tid = threadIdx.x, lane = tid & 63;
  const int wave = __builtin_amdgcn_readfirstlane(tid >> 6);
  const int grp = blockIdx.x / NSPLIT, split = blockIdx.x % NSPLIT;
  constexpr float C2 = 0.0625f * 1.4426950408889634f;

  const size_t base = (size_t)blockIdx.x * (SK / NSPLIT) * D * 2 + (size_t)wave * TL * 1024 + (TL == 8 ? 4096 : 0);
  const unsigned char* kb = kp + base;
  const unsigned char* vb = vp + base;
  const unsigned voff = lane * 16;
  const int keepk = 0, keepv = (int)((blockIdx.x + (256u - VDEF_FROM)) >> 8);

  u32x4 q[2];
  {
    const unsigned char* qb = qp + (size_t)grp * (2 * D * 2);
    const unsigned qoff = (lane & 31) * 16;
    asm volatile("global_load_dwordx4 %0, %1, %2" : "=v"(q[0]) : "v"(qoff), "s"(qb) : "memory");
    asm volatile("global_load_dwordx4 %0, %1, %2 offset:512" : "=v"(q[1]) : "v"(qoff), "s"(qb) : "memory");
  }
  u32x4 r[RS][TL];
#pragma unroll
  for (int s = 0; s < RS; ++s) issue(r[s], kb + s * ROWB, voff, keepk);
  asm volatile("s_waitcnt vmcnt(%2)" : "+v"(q[0]), "+v"(q[1]) : "i"(F) : "memory");

  float* sc = lds + wave * WLDS;
  const int h = lane >> 5;

  float mx = -INFINITY;
  for (int it = 0; it < NI; ++it) {
    const unsigned char* nxt = (it + 1 < NI) ? kb + (size_t)(it + 1) * RS * ROWB : vb;
    const int keepn = (it + 1 < NI) ? keepk : keepv;
    float x[16];
#pragma unroll
    for (int s = 0; s < RS; ++s) {
      arrive<F - TL>(r[s]);
#pragma unroll
      for (int j = 0; j < TL; ++j)
#pragma unroll
        for (int g = 0; g < 2; ++g) {
          float d = 0.f;
#pragma unroll
          for (int k = 0; k < 4; ++k) d = dot2(r[s][j][k], q[g][k], d);
          x[((s * TL + j) & 7) * 2 + g] = d;
        }
#pragma unroll
      for (int j = 0; j < TL; ++j) asm volatile("" : "+v"(x[((s * TL + j) & 7) * 2]), "+v"(x[((s * TL + j) & 7) * 2 + 1]));
      issue(r[s], nxt + s * ROWB, voff, keepn);
      if ((((s + 1) * TL) & 7) == 0) {
#define TR_STEP(HALF, CTRL, BIT)                                        \
  _Pragma("unroll") for (int i = 0; i < (HALF); ++i) {                  \
    const float lo_ = x[i], hi_ = x[i + (HALF)];                        \
    const float t1_ = lo_ + dpp<CTRL>(lo_), t2_ = hi_ + dpp<CTRL>(hi_); \
    x[i] = (lane & (BIT)) ? t2_ : t1_;                                  \
  }
        TR_STEP(8, XOR8, 8)
        TR_STEP(4, XOR7, 4)
        TR_STEP(2, XOR2, 2)
        TR_STEP(1, XOR1, 1)
#undef TR_STEP
        const float sv = swap16_sum(x[0]);
        mx = fmaxf(mx, sv);
        if ((lane & 16) == 0) sc[(it * (F / 8) + (s * TL) / 8) * 32 + h * 16 + (lane & 15)] = sv;
      }
    }
  }

  unsigned long long ts1 = __builtin_amdgcn_s_memrealtime();
  mx = fmaxf(mx, dpp<XOR2>(mx));
  mx = fmaxf(mx, dpp<ROR4>(mx));
  mx = fmaxf(mx, dpp<XOR8>(mx));
  {
    auto t = __builtin_amdgcn_permlane32_swap(__float_as_uint(mx), __float_as_uint(mx), false, false);
    mx = fmaxf(__uint_as_float(t[0]), __uint_as_float(t[1]));
  }
  const float m0 = __int_as_float(__builtin_amdgcn_readlane(__float_as_int(mx), 0)) * C2;
  const float m1 = __int_as_float(__builtin_amdgcn_readlane(__float_as_int(mx), 1)) * C2;
  float l0 = 0.f, l1 = 0.f;
#pragma unroll
  for (int i = 0; i < 4; ++i) {
    f32x4 t = *reinterpret_cast<f32x4*>(sc + i * 256 + lane * 4);
    t[0] = __builtin_amdgcn_exp2f(fmaf(t[0], C2, -m0));
    t[1] = __builtin_amdgcn_exp2f(fmaf(t[1], C2, -m1));
    t[2] = __builtin_amdgcn_exp2f(fmaf(t[2], C2, -m0));
    t[3] = __builtin_amdgcn_exp2f(fmaf(t[3], C2, -m1));
    l0 += t[0] + t[2];
    l1 += t[1] + t[3];
    *reinterpret_cast<f32x4*>(sc + i * 256 + lane * 4) = t;
  }
  l0 = wave_sum(l0);
  l1 = wave_sum(l1);

  f32x2 a0[4], a1[4];
#pragma unroll
  for (int k = 0; k < 4; ++k) { a0[k] = f32x2{0.f, 0.f}; a1[k] = f32x2{0.f, 0.f}; }
  auto pv = [&](auto nwait, int it, int s) {
    const float* pp = sc + (it * (F / 8) + (s * TL) / 8) * 32 + h * 16 + ((s * TL) & 7) * 2;
    f32x4 p[TL / 2];
#pragma unroll
    for (int j = 0; j < TL / 2; ++j) p[j] = *reinterpret_cast<const f32x4*>(pp + j * 4);
    arrive<decltype(nwait)::value>(r[s]);
#pragma unroll
    for (int j = 0; j < TL; ++j)
#pragma unroll
      for (int k = 0; k < 4; ++k) {
        const unsigned w = r[s][j][k];
        const f32x2 v = f32x2{__uint_as_float(w << 16), __uint_as_float(w & 0xffff0000u)};
        const float p0 = p[j >> 1][(j & 1) * 2], p1 = p[j >> 1][(j & 1) * 2 + 1];
        a0[k] = __builtin_elementwise_fma(v, f32x2{p0, p0}, a0[k]);
        a1[k] = __builtin_elementwise_fma(v, f32x2{p1, p1}, a1[k]);
      }
#pragma unroll
    for (int k = 0; k < 4; ++k) asm volatile("" : "+v"(a0[k]), "+v"(a1[k]));
  };
  for (int it = 0; it < NI - 1; ++it) {
    const unsigned char* nxt = vb + (size_t)(it + 1) * RS * ROWB;
#pragma unroll
    for (int s = 0; s < RS; ++s) {
      pv(IC<F - TL>{}, it, s);
      issue(r[s], nxt + s * ROWB, voff, keepv);
    }
  }
  static_for<RS>([&](auto s) { pv(IC<F - (decltype(s)::value + 1) * TL>{}, NI - 1, decltype(s)::value); });

  unsigned long long ts2 = __builtin_amdgcn_s_memrealtime();
  float o[8];
#pragma unroll
  for (int k = 0; k < 8; ++k) {
    auto t = __builtin_amdgcn_permlane32_swap(__float_as_uint(a0[k >> 1][k & 1]), __float_as_uint(a1[k >> 1][k & 1]), false, false);
    o[k] = __uint_as_float(t[0]) + __uint_as_float(t[1]);
  }
  *reinterpret_cast<f32x4*>(sc + lane * 8) = f32x4{o[0], o[1], o[2], o[3]};
  *reinterpret_cast<f32x4*>(sc + lane * 8 + 4) = f32x4{o[4], o[5], o[6], o[7]};
  if (lane == 0) *reinterpret_cast<f32x4*>(sc + 2 * D) = f32x4{m0, m1, l0, l1};
  __syncthreads();

#pragma unroll
  for (int g = 0; g < 2; ++g) {
    float M = -INFINITY;
#pragma unroll
    for (int j = 0; j < NWAVES; ++j) M = fmaxf(M, lds[j * WLDS + 2 * D + g]);
    float L = 0.f, acc = 0.f;
#pragma unroll
    for (int j = 0; j < NWAVES; ++j) {
      const float wj = __builtin_amdgcn_exp2f(lds[j * WLDS + 2 * D + g] - M);
      L = fmaf(wj, lds[j * WLDS + 2 * D + 2 + g], L);
      acc = fmaf(wj, lds[j * WLDS + g * D + tid], acc);
    }
    const size_t slot = (size_t)(grp * 2 + g) * NSPLIT + split;
    pacc[slot * D + tid] = acc;
    if (tid == 0) { pml[slot * 2] = M; pml[slot * 2 + 1] = L; }
  }
  if (lane == 0) {
    unsigned long long* sp = (unsigned long long*)((char*)pml + STAMP_OFF) + (blockIdx.x * 4 + wave) * 4;
    sp[0] = ts0; sp[1] = ts1; sp[2] = ts2; sp[3] = __builtin_amdgcn_s_memrealtime();
  }
}

__global__ void __launch_bounds__(256)
gqa_decode_combine(const float* __restrict__ pacc, const float* __restrict__ pml, __hip_bfloat16* __restrict__ out) {
  unsigned long long ts0 = __builtin_amdgcn_s_memrealtime();
  const int row = blockIdx.x, d = threadIdx.x;
  float M = -INFINITY;
#pragma unroll
  for (int s = 0; s < NSPLIT; ++s) M = fmaxf(M, pml[(row * NSPLIT + s) * 2]);
  float L = 0.f, acc = 0.f;
#pragma unroll
  for (int s = 0; s < NSPLIT; ++s) {
    const float w = __builtin_amdgcn_exp2f(pml[(row * NSPLIT + s) * 2] - M);
    L = fmaf(w, pml[(row * NSPLIT + s) * 2 + 1], L);
    acc = fmaf(w, pacc[((size_t)row * NSPLIT + s) * D + d], acc);
  }
  out[row * D + d] = __float2bfloat16(acc / L);
  if (row == 0) {
    __shared__ unsigned long long red[6][256];
    const unsigned long long* sp = (const unsigned long long*)((const char*)pml + STAMP_OFF);
    unsigned long long mn0 = ~0ull, mx1 = 0, mx3 = 0, s2lo = 0, s2hi = 0, mn2 = ~0ull;
    for (int i = d; i < 1024; i += 256) {
      const unsigned long long a = sp[i * 4], b = sp[i * 4 + 1], c = sp[i * 4 + 2], e = sp[i * 4 + 3];
      mn0 = a < mn0 ? a : mn0; mx1 = b > mx1 ? b : mx1; mx3 = e > mx3 ? e : mx3; mn2 = c < mn2 ? c : mn2;
      if (i < 512) s2lo += c; else s2hi += c;
    }
    red[0][d] = mn0; red[1][d] = mx1; red[2][d] = mx3; red[3][d] = s2lo; red[4][d] = s2hi; red[5][d] = mn2;
    __syncthreads();
    for (int o = 128; o; o >>= 1) {
      if (d < o) {
        red[0][d] = red[0][d] < red[0][d + o] ? red[0][d] : red[0][d + o];
        red[1][d] = red[1][d] > red[1][d + o] ? red[1][d] : red[1][d + o];
        red[2][d] = red[2][d] > red[2][d + o] ? red[2][d] : red[2][d + o];
        red[3][d] += red[3][d + o]; red[4][d] += red[4][d + o];
        red[5][d] = red[5][d] < red[5][d + o] ? red[5][d] : red[5][d + o];
      }
      __syncthreads();
    }
    if (d == 0) {
      long long X = 0;
      if (DIAG == 1) X = red[2][0] - red[0][0];
      if (DIAG == 2) X = red[1][0] - red[0][0];
      if (DIAG == 3) X = 4 * (long long)(ts0 - red[2][0]);
      if (DIAG == 4) X = red[3][0] / 512 - red[0][0];
      if (DIAG == 5) X = red[4][0] / 512 - red[0][0];
      if (DIAG == 6) X = red[5][0] - red[0][0];
      if (X < 0) X = 0;
      if (X > 30000) X = 30000;
      const unsigned long long tw = __builtin_amdgcn_s_memrealtime();
      while ((long long)(__builtin_amdgcn_s_memrealtime() - tw) < X) __builtin_amdgcn_s_sleep(2);
    }
  }
  if (d == 0) {
    unsigned long long* sp = (unsigned long long*)((char*)pml + STAMP_OFF) + 4096 + blockIdx.x * 2;
    sp[0] = ts0; sp[1] = __builtin_amdgcn_s_memrealtime();
  }
}

}

extern "C" void kernel_launch(void* const* d_in, const int* in_sizes, int n_in, void* d_out, int out_size, void* d_ws,
                              size_t ws_size, hipStream_t stream) {
  const unsigned char* q = (const unsigned char*)d_in[0];
  const unsigned char* k = (const unsigned char*)d_in[1];
  const unsigned char* v = (const unsigned char*)d_in[2];
  float* pml = (float*)d_ws;
  float* pacc = (float*)((char*)d_ws + 65536);
  gqa_decode_split<<<NGROUP * NSPLIT, NTHR, 0, stream>>>(q, k, v, pacc, pml);
  gqa_decode_combine<<<B * HQ, 256, 0, stream>>>(pacc, pml, (__hip_bfloat16*)d_out);
}
```

```cpp
#define TEND 110400
#define PACE0 2400
#include <hip/hip_runtime.h>
#include <hip/hip_bf16.h>
#include <stdint.h>

#define TICKET_AHEAD 4
#define PREFETCH 3
#define PACE 1740
#define KEEP 0
#define STAGW 0
#define STAGB 397

namespace {

constexpr int B = 4, HQ = 16, HKV = 8, G = HQ / HKV, SK = 16384, D = 256;
constexpr int NGROUP = B * HKV;
constexpr int NSPLIT = 8, NWAVES = 4, NTHR = NWAVES * 64;
constexpr int TL = 8;
constexpr int NI = SK / (NSPLIT * NWAVES * 2 * TL);
constexpr unsigned ROWB = NWAVES * TL * 1024;
constexpr int IT_TICKET = NI - TICKET_AHEAD;
static_assert(G == 2, "layout assumes 2 query heads per kv head");
static_assert(IT_TICKET >= 0 && IT_TICKET + 2 <= NI - 2 && NI % 2 == 0, "ticket turn");

constexpr unsigned CTL_STRIDE = 128, REC_BASE = 8192, REC_STRIDE = 4224;

typedef unsigned u32x2 __attribute__((ext_vector_type(2)));
typedef unsigned u32x4 __attribute__((ext_vector_type(4)));
typedef float f32x4 __attribute__((ext_vector_type(4)));
typedef float f32x2 __attribute__((ext_vector_type(2)));
typedef __bf16 bf16x2 __attribute__((ext_vector_type(2)));
typedef __attribute__((address_space(3))) int lds_int;

__device__ __forceinline__ void flag_set(lds_int* p, int v) { __hip_atomic_store(p, v, __ATOMIC_RELAXED, __HIP_MEMORY_SCOPE_WORKGROUP); }
__device__ __forceinline__ int flag_get(lds_int* p) {
  return __builtin_amdgcn_readfirstlane(__hip_atomic_load(p, __ATOMIC_RELAXED, __HIP_MEMORY_SCOPE_WORKGROUP));
}
__device__ __forceinline__ float dot2(unsigned a, unsigned b, float acc) {
  return __builtin_amdgcn_fdot2_f32_bf16(__builtin_bit_cast(bf16x2, a), __builtin_bit_cast(bf16x2, b), acc, false);
}
template <int CTRL> __device__ __forceinline__ float dpp(float x) {
  return __builtin_bit_cast(float, __builtin_amdgcn_mov_dpp(__builtin_bit_cast(int, x), CTRL, 0xf, 0xf, true));
}
constexpr int XOR1 = 0xB1, XOR2 = 0x4E, XOR7 = 0x141, XOR8 = 0x128, ROR4 = 0x124, BCAST = 0x150;

__device__ __forceinline__ float swap16_sum(float x) {
  auto r = __builtin_amdgcn_permlane16_swap(__float_as_uint(x), __float_as_uint(x), false, false);
  return __uint_as_float(r[0]) + __uint_as_float(r[1]);
}
__device__ __forceinline__ float swap32_sum(float x) {
  auto r = __builtin_amdgcn_permlane32_swap(__float_as_uint(x), __float_as_uint(x), false, false);
  return __uint_as_float(r[0]) + __uint_as_float(r[1]);
}
__device__ __forceinline__ float wave_sum(float x) {
  x += dpp<XOR1>(x); x += dpp<XOR2>(x); x += dpp<ROR4>(x); x += dpp<XOR8>(x);
  return swap32_sum(swap16_sum(x));
}

#define R8_(C, r) C(r[0]), C(r[1]), C(r[2]), C(r[3]), C(r[4]), C(r[5]), C(r[6]), C(r[7])
#define LD_(R, OFF) "global_load_dwordx4 %" #R ", %[vo], %[sb] offset:" #OFF " nt\n\t"
#define LD8_ LD_(0, -4096) LD_(1, -3072) LD_(2, -2048) LD_(3, -1024) LD_(4, 0) LD_(5, 1024) LD_(6, 2048) LD_(7, 3072)
__device__ __forceinline__ void issue(u32x4 (&r)[TL], const unsigned char* base, unsigned voff) {
  asm volatile(LD8_ : R8_("=&v", r) : [vo] "v"(voff), [sb] "s"(base) : "memory");
}
__device__ __forceinline__ void issue_if(u32x4 (&r)[TL], const unsigned char* base, unsigned voff, int go) {
  asm volatile("s_cmp_eq_u32 %[go], 0\n\ts_cbranch_scc1 1f\n\t" LD8_ "1:"
               : R8_("+v", r) : [vo] "v"(voff), [sb] "s"(base), [go] "s"(go) : "memory", "scc");
}
__device__ __forceinline__ void arrive(u32x4 (&r)[TL]) { asm volatile("s_waitcnt vmcnt(8)" : R8_("+v", r) :: "memory"); }
__device__ __forceinline__ void arrive_keep(u32x4 (&r)[TL], int keep) {
  asm volatile("s_waitcnt vmcnt(9)\n\ts_cmp_eq_u32 %[k], 9\n\ts_cbranch_scc1 1f\n\t"
               "s_waitcnt vmcnt(8)\n\ts_cmp_eq_u32 %[k], 8\n\ts_cbranch_scc1 1f\n\t"
               "s_waitcnt vmcnt(0)\n1:"
               : R8_("+v", r) : [k] "s"(keep) : "memory", "scc");
}

#define RL_(R, O) "global_load_dwordx4 %" #R ", %[" #O "], %[sb] sc1\n\t"
__device__ __forceinline__ void rec_issue(u32x4 (&rc)[9], const unsigned (&off)[9], const unsigned char* base, int go) {
  asm volatile("s_cmp_eq_u32 %[go], 0\n\ts_cbranch_scc1 1f\n\t"
               RL_(8, o8) RL_(0, o0) RL_(1, o1) RL_(2, o2) RL_(3, o3) RL_(4, o4) RL_(5, o5) RL_(6, o6) RL_(7, o7) "1:"
               : R8_("+v", rc), "+v"(rc[8])
               : [o0] "v"(off[0]), [o1] "v"(off[1]), [o2] "v"(off[2]), [o3] "v"(off[3]), [o4] "v"(off[4]), [o5] "v"(off[5]),
                 [o6] "v"(off[6]), [o7] "v"(off[7]), [o8] "v"(off[8]), [sb] "s"(base), [go] "s"(go)
               : "memory", "scc");
}
__device__ __forceinline__ void rec_arrive(u32x4 (&rc)[9]) {
  asm volatile("s_waitcnt vmcnt(0)" : R8_("+v", rc), "+v"(rc[8]) :: "memory");
}

template <int N> struct IC { static constexpr int value = N; };
template <int N, int I = 0, class Fn> __device__ __forceinline__ void static_for(Fn&& fn) {
  if constexpr (I < N) { fn(IC<I>{}); static_for<N, I + 1>(fn); }
}

constexpr int WLDS = 1024;

__global__ void __launch_bounds__(NTHR, 1)
gqa_decode(const unsigned char* __restrict__ qp, const unsigned char* __restrict__ kp,
           const unsigned char* __restrict__ vp, unsigned char* __restrict__ ws, __hip_bfloat16* __restrict__ out) {
  __shared__ __attribute__((aligned(16))) float lds[NWAVES * WLDS + 4];
  const unsigned start = (unsigned)__builtin_amdgcn_s_memrealtime() * 256u;
  __builtin_amdgcn_sched_barrier(0);
  const int tid = threadIdx.x, lane = tid & 63;
  const int wave = __builtin_amdgcn_readfirstlane(tid >> 6);
  const int grp = blockIdx.x / NSPLIT, split = blockIdx.x % NSPLIT;
  constexpr float C2 = 0.0625f * 1.4426950408889634f;

  const size_t base = (size_t)blockIdx.x * (SK / NSPLIT) * D * 2 + (size_t)wave * TL * 1024 + 4096;
  const unsigned char* kb = kp + base;
  const unsigned char* vb = vp + base;
  const unsigned voff = lane * 16;
  unsigned char* ctl = ws + grp * CTL_STRIDE;
  lds_int* lflag = (lds_int*)(lds + NWAVES * WLDS);

  u32x4 r[2][TL];
  u32x4 q[2];
  unsigned epoch;
  u32x2 tk;
  const unsigned zero = 0;
  {
    const unsigned char* qb = qp + (size_t)grp * (2 * D * 2);
    const unsigned qoff = (lane & 31) * 16;
    asm volatile("global_load_dwordx4 %0, %4, %5\n\t"
                 "global_load_dwordx4 %1, %4, %5 offset:512\n\t"
                 "global_load_dword %2, %6, %7 sc1\n\t"
                 "global_load_dwordx2 %3, %6, %7 offset:64 sc1"
                 : "=&v"(q[0]), "=&v"(q[1]), "=&v"(epoch), "=&v"(tk) : "v"(qoff), "s"(qb), "v"(zero), "s"(ctl) : "memory");
  }
  issue(r[0], kb, voff);
  issue(r[1], kb + ROWB, voff);
  const unsigned late = (unsigned)__builtin_amdgcn_s_memrealtime() * 256u - start;
  unsigned due = start + late + (PACE0 + ((blockIdx.x * STAGB + wave * STAGW) & 1023) * PACE / 1024) * 16u;
  int step = (int)((TEND - PACE0) * 16u - late / 256u * (256u - KEEP)) / (2 * NI - 3);
  step = step < (PACE - 100) * 16 ? (PACE - 100) * 16 : step;
  auto pace = [&]() {
    while ((int)((unsigned)__builtin_amdgcn_s_memrealtime() * 256u - due) < 0) __builtin_amdgcn_s_sleep(1);
    due += step;
  };
  if (tid == 0) flag_set(lflag, 0);
  asm volatile("s_waitcnt lgkmcnt(0)\n\ts_barrier" ::: "memory");
  asm volatile("s_waitcnt vmcnt(16)" : "+v"(q[0]), "+v"(q[1]), "+v"(epoch), "+v"(tk) :: "memory");
#define TLINE()

  epoch = __builtin_amdgcn_readfirstlane(epoch);
  const unsigned tagA = epoch * 0x9E3779B1u + 0x7F4A7C15u, tagB = ~tagA;

  float* sc = lds + wave * WLDS;
  const int h = lane >> 5;

  float mx = -INFINITY;
#pragma unroll 1
  for (int it = 0; it < NI; it += 2) {
    const unsigned char* nxt = (it + 2 < NI) ? kb + (size_t)(it + 2) * ROWB : vb;
#pragma unroll
    for (int s = 0; s < 2; ++s) {
      float x[16];
      arrive(r[s]);
      TLINE();
#pragma unroll
      for (int j = 0; j < TL; ++j)
#pragma unroll
        for (int g = 0; g < 2; ++g) {
          float d = 0.f;
#pragma unroll
          for (int k = 0; k < 4; ++k) d = dot2(r[s][j][k], q[g][k], d);
          x[j * 2 + g] = d;
        }
#define TR_STEP(HALF, CTRL, BIT)                                        \
  _Pragma("unroll") for (int i = 0; i < (HALF); ++i) {                  \
    const float lo_ = x[i], hi_ = x[i + (HALF)];                        \
    const float t1_ = lo_ + dpp<CTRL>(lo_), t2_ = hi_ + dpp<CTRL>(hi_); \
    x[i] = (lane & (BIT)) ? t2_ : t1_;                                  \
  }
      TR_STEP(8, XOR8, 8)
      TR_STEP(4, XOR7, 4)
      TR_STEP(2, XOR2, 2)
      TR_STEP(1, XOR1, 1)
#undef TR_STEP
      float sv = swap16_sum(x[0]);
      mx = fmaxf(mx, sv);
      if ((lane & 16) == 0) sc[(it + s) * 32 + h * 16 + (lane & 15)] = sv;
      asm volatile("" : "+v"(mx));
      pace();
      issue(r[s], nxt + s * ROWB, voff);
    }
  }
  if (tid == 0 && (tk[1] != tagA || ((tk[0] ^ tagB) >> 8) != 0)) {
    u32x4 cs = {tagB & 0xffffff00u, tagA, tk[0], tk[1]};
    asm volatile("global_atomic_cmpswap_x2 %0, %1, %2 offset:64\n\ts_nop 1" :: "v"(zero), "v"(cs), "s"(ctl) : "memory");
  }

  mx = fmaxf(mx, dpp<XOR2>(mx));
  mx = fmaxf(mx, dpp<ROR4>(mx));
  mx = fmaxf(mx, dpp<XOR8>(mx));
  {
    auto t = __builtin_amdgcn_permlane32_swap(__float_as_uint(mx), __float_as_uint(mx), false, false);
    mx = fmaxf(__uint_as_float(t[0]), __uint_as_float(t[1]));
  }
  const float m0 = __int_as_float(__builtin_amdgcn_readlane(__float_as_int(mx), 0)) * C2;
  const float m1 = __int_as_float(__builtin_amdgcn_readlane(__float_as_int(mx), 1)) * C2;
  float l0 = 0.f, l1 = 0.f;
#pragma unroll
  for (int i = 0; i < 4; ++i) {
    f32x4 t = *reinterpret_cast<f32x4*>(sc + i * 256 + lane * 4);
    t[0] = __builtin_amdgcn_exp2f(fmaf(t[0], C2, -m0));
    t[1] = __builtin_amdgcn_exp2f(fmaf(t[1], C2, -m1));
    t[2] = __builtin_amdgcn_exp2f(fmaf(t[2], C2, -m0));
    t[3] = __builtin_amdgcn_exp2f(fmaf(t[3], C2, -m1));
    l0 += t[0] + t[2];
    l1 += t[1] + t[3];
    *reinterpret_cast<f32x4*>(sc + i * 256 + lane * 4) = t;
  }
  l0 = wave_sum(l0);
  l1 = wave_sum(l1);

  const unsigned char* rbase = ws + REC_BASE + (size_t)grp * (NSPLIT * REC_STRIDE);
  unsigned roff[9];
  u32x4 rc[9];
#pragma unroll
  for (int s = 0; s < 8; ++s) { roff[s] = s * REC_STRIDE + tid * 16; rc[s] = u32x4{0, 0, 0, 0}; }
  roff[8] = ((lane >> 1) & 7) * REC_STRIDE + 4096 + (lane & 1) * 16;
  rc[8] = u32x4{0, 0, 0, 0};
  int early = 0;
  unsigned ticket = 1;
  f32x2 a0[4], a1[4];
#pragma unroll
  for (int k = 0; k < 4; ++k) { a0[k] = f32x2{0.f, 0.f}; a1[k] = f32x2{0.f, 0.f}; }
  int keep = 8;
#pragma unroll 1
  for (int it2 = 0; it2 < NI; it2 += 2) {
#pragma unroll
    for (int s = 0; s < 2; ++s) {
      const int it = it2 + s;
      const float* pp = sc + it * 32 + h * 16;
      f32x4 p[TL / 2];
#pragma unroll
      for (int j = 0; j < TL / 2; ++j) p[j] = *reinterpret_cast<const f32x4*>(pp + j * 4);
      arrive_keep(r[s], keep);
      TLINE();
#pragma unroll
      for (int j = 0; j < TL; ++j)
#pragma unroll
        for (int k = 0; k < 4; ++k) {
          const unsigned w = r[s][j][k];
          const f32x2 v = f32x2{__uint_as_float(w << 16), __uint_as_float(w & 0xffff0000u)};
          const float p0 = p[j >> 1][(j & 1) * 2], p1 = p[j >> 1][(j & 1) * 2 + 1];
          a0[k] = __builtin_elementwise_fma(v, f32x2{p0, p0}, a0[k]);
          a1[k] = __builtin_elementwise_fma(v, f32x2{p1, p1}, a1[k]);
        }
#pragma unroll
      for (int k = 0; k < 4; ++k) asm volatile("" : "+v"(a0[k]), "+v"(a1[k]));
      if (it2 < NI - 2) pace();
      issue_if(r[s], vb + (size_t)(it + 2) * ROWB, voff, NI - 2 - it2);
      keep = 8;
      if (s == (IT_TICKET & 1)) {
        unsigned long long sv;
        const int no = wave | (it ^ IT_TICKET);
        asm volatile("s_cmp_lg_u32 %[no], 0\n\ts_cbranch_scc1 1f\n\t"
                     "s_mov_b64 %[sv], exec\n\ts_mov_b64 exec, 1\n\t"
                     "global_atomic_add %[t], %[z], %[t], %[ctl] offset:64 sc0\n\t"
                     "s_mov_b64 exec, %[sv]\n1:"
                     : [t] "+v"(ticket), [sv] "=&s"(sv) : [no] "s"(no), [z] "v"(zero), [ctl] "s"(ctl)
                     : "memory", "scc");
        keep = no ? 8 : 9;
        if (it == IT_TICKET + 2 && tid == 0) flag_set(lflag, 1 + ((ticket & 7) == 7));
      }
      if (s == 0) {
        early = (PREFETCH & 1) && it == NI - 2 ? flag_get(lflag) >> 1 : 0;
        rec_issue(rc, roff, rbase, early);
        keep = it == NI - 2 ? early * 9 : keep;
      }
    }
  }
  if (PREFETCH & 2) {
    const int now = (flag_get(lflag) >> 1) & ~early;
    rec_issue(rc, roff, rbase, now);
    early |= now;
  }

  float o[8];
#pragma unroll
  for (int k = 0; k < 8; ++k) {
    auto t = __builtin_amdgcn_permlane32_swap(__float_as_uint(a0[k >> 1][k & 1]), __float_as_uint(a1[k >> 1][k & 1]), false, false);
    o[k] = __uint_as_float(t[0]) + __uint_as_float(t[1]);
  }
  *reinterpret_cast<f32x4*>(sc + lane * 8) = f32x4{o[0], o[1], o[2], o[3]};
  *reinterpret_cast<f32x4*>(sc + lane * 8 + 4) = f32x4{o[4], o[5], o[6], o[7]};
  if (lane == 0) *reinterpret_cast<f32x4*>(sc + 2 * D) = f32x4{m0, m1, l0, l1};
  asm volatile("s_waitcnt lgkmcnt(0)\n\ts_barrier" ::: "memory");

  float M[2], L[2], acc[2];
#pragma unroll
  for (int g = 0; g < 2; ++g) {
    M[g] = -INFINITY;
#pragma unroll
    for (int j = 0; j < NWAVES; ++j) M[g] = fmaxf(M[g], lds[j * WLDS + 2 * D + g]);
    L[g] = 0.f; acc[g] = 0.f;
#pragma unroll
    for (int j = 0; j < NWAVES; ++j) {
      const float wj = __builtin_amdgcn_exp2f(lds[j * WLDS + 2 * D + g] - M[g]);
      L[g] = fmaf(wj, lds[j * WLDS + 2 * D + 2 + g], L[g]);
      acc[g] = fmaf(wj, lds[j * WLDS + g * D + tid], acc[g]);
    }
  }
  const int last = flag_get(lflag) >> 1;
  if (!last) {
    unsigned char* mine = ws + REC_BASE + (size_t)blockIdx.x * REC_STRIDE;
    const u32x4 pc = {__float_as_uint(acc[0]), tagA, __float_as_uint(acc[1]), tagB};
    const unsigned o1 = tid * 16;
    asm volatile("global_store_dwordx4 %0, %1, %2 sc1\n\ts_nop 1" :: "v"(o1), "v"(pc), "s"(mine) : "memory");
    if (tid < 2) {
      const u32x4 ml = {__float_as_uint(tid ? M[1] : M[0]), tagA, __float_as_uint(tid ? L[1] : L[0]), tagB};
      const unsigned o2 = 4096 + tid * 16;
      asm volatile("global_store_dwordx4 %0, %1, %2 sc1\n\ts_nop 1" :: "v"(o2), "v"(ml), "s"(mine) : "memory");
    }
  } else {
    const int ms = (lane >> 1) & 7;
    int go = early ^ 1;
#pragma unroll 1
    for (int spin = 0; spin < (1 << 20); ++spin) {
      rec_issue(rc, roff, rbase, go);
      rec_arrive(rc);
      unsigned bad = ms == split ? 0u : (rc[8][1] ^ tagA) | (rc[8][3] ^ tagB);
#pragma unroll
      for (int s = 0; s < 8; ++s) bad |= ((rc[s][1] ^ tagA) | (rc[s][3] ^ tagB)) & (s == split ? 0u : ~0u);
      if (__ballot(bad != 0) == 0) break;
      go = 1;
      __builtin_amdgcn_s_sleep(2);
    }
    const float mm = ms == split ? ((lane & 1) ? M[1] : M[0]) : __uint_as_float(rc[8][0]);
    const float ll = ms == split ? ((lane & 1) ? L[1] : L[0]) : __uint_as_float(rc[8][2]);
    float gm = fmaxf(mm, dpp<XOR2>(mm));
    gm = fmaxf(gm, dpp<ROR4>(gm));
    gm = fmaxf(gm, dpp<XOR8>(gm));
    const float w = __builtin_amdgcn_exp2f(mm - gm);
    float gl = w * ll;
    gl += dpp<XOR2>(gl);
    gl += dpp<ROR4>(gl);
    gl += dpp<XOR8>(gl);
    const float wn = w / gl;
    float r0 = 0.f, r1 = 0.f;
    static_for<8>([&](auto sI) {
      constexpr int s = decltype(sI)::value;
      const float x0 = s == split ? acc[0] : __uint_as_float(rc[s][0]);
      const float x1 = s == split ? acc[1] : __uint_as_float(rc[s][2]);
      r0 = fmaf(dpp<BCAST + 2 * s>(wn), x0, r0);
      r1 = fmaf(dpp<BCAST + 2 * s + 1>(wn), x1, r1);
    });
    out[(grp * 2) * D + tid] = __float2bfloat16(r0);
    out[(grp * 2 + 1) * D + tid] = __float2bfloat16(r1);
    if (tid == 0) {
      const unsigned next = epoch + 1;
      asm volatile("global_store_dword %0, %1, %2 sc1\n\ts_nop 1" :: "v"(zero), "v"(next), "s"(ctl) : "memory");
    }
  }
}

}

extern "C" void kernel_launch(void* const* d_in, const int* in_sizes, int n_in, void* d_out, int out_size, void* d_ws,
                              size_t ws_size, hipStream_t stream) {
  gqa_decode<<<NGROUP * NSPLIT, NTHR, 0, stream>>>((const unsigned char*)d_in[0], (const unsigned char*)d_in[1],
                                                   (const unsigned char*)d_in[2], (unsigned char*)d_ws,
                                                   (__hip_bfloat16*)d_out);
}
```

```cpp
#include <hip/hip_runtime.h>
#include <hip/hip_bf16.h>
#include <stdint.h>

#define PACE 1740
#define PACE0 2400
#define TEND 110400
#define TEND2 108300
#define CLAG 0
#define TOFF 1700
#define SYNC 1
#define NAP 1
#define PROG 1

namespace {

constexpr int B = 4, HQ = 16, HKV = 8, G = HQ / HKV, SK = 16384, D = 256;
constexpr int NGROUP = B * HKV;
constexpr int NSPLIT = 8, NWAVES = 4, NTHR = 2 * NWAVES * 64;
constexpr int TL = 8;
constexpr int NI = SK / (NSPLIT * NWAVES * 2 * TL);
constexpr unsigned ROWB = NWAVES * TL * 1024;
constexpr int IT_SYNC = 8;
constexpr int SPIN = 1 << 20;
static_assert(G == 2, "layout assumes 2 query heads per kv head");

constexpr unsigned CTL_STRIDE = 128, CLK_BASE = 4096, ML_BASE = 8192, ML_STRIDE = 256, REC_BASE = 65536, REC_STRIDE = 4096;

typedef unsigned u32x4 __attribute__((ext_vector_type(4)));
typedef float f32x4 __attribute__((ext_vector_type(4)));
typedef float f32x2 __attribute__((ext_vector_type(2)));
typedef __bf16 bf16x2 __attribute__((ext_vector_type(2)));
typedef __attribute__((address_space(3))) int lds_int;

__device__ __forceinline__ void flag_set(lds_int* p, int v) { __hip_atomic_store(p, v, __ATOMIC_RELAXED, __HIP_MEMORY_SCOPE_WORKGROUP); }
__device__ __forceinline__ int flag_get(lds_int* p) {
  return __builtin_amdgcn_readfirstlane(__hip_atomic_load(p, __ATOMIC_RELAXED, __HIP_MEMORY_SCOPE_WORKGROUP));
}
__device__ __forceinline__ void flag_bump(lds_int* p) {
  const int one = 1;
  asm volatile("s_mov_b64 exec, 1\n\tds_add_u32 %0, %1\n\ts_mov_b64 exec, -1" :: "v"(p), "v"(one) : "memory");
}
__device__ __forceinline__ unsigned clock256() { return (unsigned)__builtin_amdgcn_s_memrealtime() * 256u; }
__device__ __forceinline__ float dot2(unsigned a, unsigned b, float acc) {
  return __builtin_amdgcn_fdot2_f32_bf16(__builtin_bit_cast(bf16x2, a), __builtin_bit_cast(bf16x2, b), acc, false);
}
template <int CTRL> __device__ __forceinline__ float dpp(float x) {
  return __builtin_bit_cast(float, __builtin_amdgcn_mov_dpp(__builtin_bit_cast(int, x), CTRL, 0xf, 0xf, true));
}
constexpr int XOR1 = 0xB1, XOR2 = 0x4E, XOR7 = 0x141, XOR8 = 0x128, ROR4 = 0x124, BCAST = 0x150;

__device__ __forceinline__ float swap16_sum(float x) {
  auto r = __builtin_amdgcn_permlane16_swap(__float_as_uint(x), __float_as_uint(x), false, false);
  return __uint_as_float(r[0]) + __uint_as_float(r[1]);
}
__device__ __forceinline__ float swap32_sum(float x) {
  auto r = __builtin_amdgcn_permlane32_swap(__float_as_uint(x), __float_as_uint(x), false, false);
  return __uint_as_float(r[0]) + __uint_as_float(r[1]);
}
__device__ __forceinline__ float swap32_max(float x) {
  auto r = __builtin_amdgcn_permlane32_swap(__float_as_uint(x), __float_as_uint(x), false, false);
  return fmaxf(__uint_as_float(r[0]), __uint_as_float(r[1]));
}
__device__ __forceinline__ float wave_sum(float x) {
  x += dpp<XOR1>(x); x += dpp<XOR2>(x); x += dpp<ROR4>(x); x += dpp<XOR8>(x);
  return swap32_sum(swap16_sum(x));
}
__device__ __forceinline__ float lane_of(float x, int l) { return __int_as_float(__builtin_amdgcn_readlane(__float_as_int(x), l)); }

#define R8_(C, r) C(r[0]), C(r[1]), C(r[2]), C(r[3]), C(r[4]), C(r[5]), C(r[6]), C(r[7])
#define LD_(R, OFF) "global_load_dwordx4 %" #R ", %[vo], %[sb] offset:" #OFF " nt\n\t"
#define LD8_ LD_(0, -4096) LD_(1, -3072) LD_(2, -2048) LD_(3, -1024) LD_(4, 0) LD_(5, 1024) LD_(6, 2048) LD_(7, 3072)
__device__ __forceinline__ void issue(u32x4 (&r)[TL], const unsigned char* base, unsigned voff) {
  asm volatile(LD8_ : R8_("=&v", r) : [vo] "v"(voff), [sb] "s"(base) : "memory");
}
__device__ __forceinline__ void issue_if(u32x4 (&r)[TL], const unsigned char* base, unsigned voff, int go) {
  asm volatile("s_cmp_eq_u32 %[go], 0\n\ts_cbranch_scc1 1f\n\t" LD8_ "1:"
               : R8_("+v", r) : [vo] "v"(voff), [sb] "s"(base), [go] "s"(go) : "memory", "scc");
}
__device__ __forceinline__ void arrive(u32x4 (&r)[TL]) { asm volatile("s_waitcnt vmcnt(8)" : R8_("+v", r) :: "memory"); }
template <int J> __device__ __forceinline__ void arrive_one(u32x4& r, int more) {
  asm volatile("s_waitcnt vmcnt(%2)\n\ts_cmp_lg_u32 %1, 0\n\ts_cbranch_scc1 1f\n\ts_waitcnt vmcnt(%3)\n1:"
               : "+v"(r) : "s"(more), "n"(15 - J), "n"(7 - J) : "memory", "scc");
}

template <int N> struct IC { static constexpr int value = N; };
template <int N, int I = 0, class Fn> __device__ __forceinline__ void static_for(Fn&& fn) {
  if constexpr (I < N) { fn(IC<I>{}); static_for<N, I + 1>(fn); }
}

constexpr int WLDS = 1024;
enum { C_ML = 0, C_DONE = 1, C_READY = 2, C_END = 3, C_TAG = 4, C_FAC = 8, C_MLV = 16, C_REC = 32, C_WORDS = 48 };
#define S_(x) #x
#define STR_(x) S_(x)

__global__ void __launch_bounds__(NTHR, 2)
gqa_decode(const unsigned char* __restrict__ qp, const unsigned char* __restrict__ kp,
           const unsigned char* __restrict__ vp, unsigned char* __restrict__ ws, unsigned char* __restrict__ out) {
  __shared__ __attribute__((aligned(16))) float lds[NWAVES * WLDS + C_WORDS];
  const unsigned long long start64 = __builtin_amdgcn_s_memrealtime();
  __builtin_amdgcn_sched_barrier(0);
  const int tid = threadIdx.x, lane = tid & 63;
  const int wave = __builtin_amdgcn_readfirstlane(tid >> 6);
  const int grp = (blockIdx.x & 7) * 4 + ((blockIdx.x >> 3) & 3), split = blockIdx.x >> 5;
  constexpr float C2 = 0.0625f * 1.4426950408889634f;
  lds_int* cw = (lds_int*)(lds + NWAVES * WLDS);
  const unsigned voff = lane * 16;
  const unsigned stag = blockIdx.x * PACE / 16;
  const unsigned base = (unsigned)(grp * NSPLIT + split) * ((SK / NSPLIT) * D * 2) + (wave & 3) * (TL * 1024) + 4096;
  const unsigned qoff = (lane & 31) * 16 + grp * (2 * D * 2);
  asm volatile("" :: "s"(qp), "s"(kp), "s"(vp), "s"(ws), "s"(out), "s"(base), "s"(stag), "v"(qoff), "v"(voff), "s"(wave));
  const unsigned start = (unsigned)start64 * 256u;

  if (__builtin_expect(wave >= NWAVES, 0)) {
  const int t = tid - NWAVES * 64;
  const unsigned zero = 0;
  const bool adder = split == NSPLIT - 1;
  unsigned end = start + TEND * 16u + stag + (adder ? CLAG * 16u : 0u);
  if (wave == NWAVES) {
    unsigned char* ctl = ws + grp * CTL_STRIDE;
    unsigned epoch;
    asm volatile("global_load_dword %0, %1, %2 sc1" : "=&v"(epoch) : "v"(zero), "s"(ctl) : "memory");
    if (lane < 8) flag_set(cw + lane, 0);
    asm volatile("s_waitcnt lgkmcnt(0)\n\ts_barrier" ::: "memory");
    if (SYNC && blockIdx.x < 64 && lane == 0) {
      const unsigned long long now = __builtin_amdgcn_s_memrealtime();
      const unsigned o = CLK_BASE + blockIdx.x * 8;
      asm volatile("global_store_dwordx2 %0, %1, %2 sc1" :: "v"(o), "v"(now), "s"(ws) : "memory");
    }
    asm volatile("s_waitcnt vmcnt(0)" : "+v"(epoch) :: "memory");
    epoch = __builtin_amdgcn_readfirstlane(epoch);
    const unsigned tagA = epoch * 0x9E3779B1u + 0x7F4A7C15u, tagB = ~tagA;
    if (SYNC) {
      while ((int)(clock256() - start) < 500 * 256) __builtin_amdgcn_s_sleep(16);
      unsigned long long c;
      const unsigned o = CLK_BASE + lane * 8;
      asm volatile("global_load_dwordx2 %0, %1, %2 sc1\n\ts_waitcnt vmcnt(0)" : "=&v"(c) : "v"(o), "s"(ws) : "memory");
      const unsigned long long d = c - start64 + 512;
      const bool ok = d < 1024;
      const float sum = wave_sum(ok ? (float)(unsigned)d : 0.f);
      const int n = __builtin_popcountll(__builtin_amdgcn_ballot_w64(ok));
      if (n >= 32) {
        end += (TEND2 - TEND) * 16u + (__builtin_amdgcn_readfirstlane((int)(sum * 256.f / (float)n)) - 512 * 256);
        if (lane == 0) flag_set(cw + C_END, end | 1);
      }
    }
    for (int spin = 0; spin < SPIN && flag_get(cw + C_ML) < NWAVES; ++spin) __builtin_amdgcn_s_sleep(32);
    asm volatile("" ::: "memory");
    const f32x2 own = *reinterpret_cast<const f32x2*>(lds + NWAVES * WLDS + C_MLV + (lane & 7) * 2);
    float mm = fmaxf(own[0], dpp<XOR2>(own[0]));
    mm = fmaxf(mm, dpp<ROR4>(mm));
    float fac = __builtin_amdgcn_exp2f(own[0] - mm);
    float ll = fac * own[1];
    ll += dpp<XOR2>(ll);
    ll += dpp<ROR4>(ll);
    unsigned char* mlb = ws + ML_BASE + grp * ML_STRIDE;
    if (!adder) {
      if (lane < 2) {
        const u32x4 ml = {__float_as_uint(mm), tagA, __float_as_uint(ll), tagB};
        const unsigned o = split * 32 + voff;
        asm volatile("global_store_dwordx4 %0, %1, %2 sc1\n\ts_nop 1" :: "v"(o), "v"(ml), "s"(mlb) : "memory");
      }
    } else {
      u32x4 ml;
      const unsigned o = (lane & 15) * 16;
      const bool mine = (lane & 15) >= 14;
#pragma unroll 1
      for (int spin = 0; spin < SPIN; ++spin) {
        __builtin_amdgcn_s_sleep(64);
        asm volatile("global_load_dwordx4 %0, %1, %2 sc1\n\ts_waitcnt vmcnt(0)" : "=&v"(ml) : "v"(o), "s"(mlb) : "memory");
        if (__builtin_amdgcn_ballot_w64(!mine && (ml[1] != tagA || ml[3] != tagB)) == 0) break;
      }
      if (lane == 0) {
        const unsigned next = epoch + 1;
        asm volatile("global_store_dword %0, %1, %2 sc1" :: "v"(zero), "v"(next), "s"(ctl) : "memory");
      }
      mm = mine ? mm : __uint_as_float(ml[0]);
      ll = mine ? ll : __uint_as_float(ml[2]);
      float gm = fmaxf(mm, dpp<XOR2>(mm));
      gm = fmaxf(gm, dpp<ROR4>(gm));
      gm = fmaxf(gm, dpp<XOR8>(gm));
      const float w = __builtin_amdgcn_exp2f(mm - gm);
      float gl = w * ll;
      gl += dpp<XOR2>(gl);
      gl += dpp<ROR4>(gl);
      gl += dpp<XOR8>(gl);
      const float wn = w / gl;
      if (lane < 16) lds[NWAVES * WLDS + C_REC + lane] = wn;
      const float w14 = dpp<BCAST + 14>(wn), w15 = dpp<BCAST + 15>(wn);
      fac *= (lane & 1) ? w15 : w14;
    }
    if (lane < 8) lds[NWAVES * WLDS + C_FAC + lane] = fac;
    asm volatile("" ::: "memory");
    if (lane == 0) { flag_set(cw + C_TAG, tagA); flag_set(cw + C_END, end | 1); flag_set(cw + C_READY, 1); }
  } else {
    asm volatile("s_barrier" ::: "memory");
    while ((int)(clock256() - end) < -6 * PACE * 16) __builtin_amdgcn_s_sleep(64);
  }
  for (int spin = 0; spin < SPIN && flag_get(cw + C_READY) == 0; ++spin) __builtin_amdgcn_s_sleep(8);
  asm volatile("" ::: "memory");
  const unsigned tagA = flag_get(cw + C_TAG), tagB = ~tagA;
  end = flag_get(cw + C_END);
  const unsigned t4 = ((t >> 3) * 4 + (t & 3) + ((t >> 2) & 1) * 272) * 4, t16 = t * 16;

  if (!adder) {
    unsigned char* mine = ws + REC_BASE + (size_t)(grp * NSPLIT + split) * REC_STRIDE;
    while ((int)(clock256() - end) < 0) __builtin_amdgcn_s_sleep(4);
    int left, cnt;
    asm volatile(
        "ds_read_b128 v[108:111], %[zero] offset:%[c_fac]\n\t"
        "ds_read_b128 v[112:115], %[zero] offset:%[c_fac]+16\n\t"
        "v_mov_b32 v49, %[tagA]\n\t"
        "v_mov_b32 v51, %[tagB]\n\t"
        "s_mov_b32 %[left], 0x100000\n"
        "1:\n\t"
        "s_sub_u32 %[left], %[left], 1\n\t"
        "s_cbranch_scc1 2f\n\t"
        "ds_read_b32 v124, %[zero] offset:%[c_done]\n\t"
        "ds_read2st64_b32 v[116:117], %[t4] offset1:2\n\t"
        "ds_read2st64_b32 v[118:119], %[t4] offset0:16 offset1:18\n\t"
        "ds_read2st64_b32 v[120:121], %[t4] offset0:32 offset1:34\n\t"
        "ds_read2st64_b32 v[122:123], %[t4] offset0:48 offset1:50\n\t"
        "s_waitcnt lgkmcnt(4)\n\t"
        "v_readfirstlane_b32 %[cnt], v124\n\t"
        "s_waitcnt lgkmcnt(0)\n\t"
        "v_pk_mul_f32 v[118:119], v[110:111], v[118:119]\n\t"
        "v_pk_mul_f32 v[122:123], v[114:115], v[122:123]\n\t"
        "s_cmp_lg_u32 %[cnt], 4\n\t"
        "s_cbranch_scc1 1b\n"
        "2:\n\t"
        "v_pk_fma_f32 v[116:117], v[108:109], v[116:117], v[118:119]\n\t"
        "v_pk_fma_f32 v[120:121], v[112:113], v[120:121], v[122:123]\n\t"
        "v_add_f32 v48, v116, v120\n\t"
        "v_add_f32 v50, v117, v121\n\t"
        "global_store_dwordx4 %[t16], v[48:51], %[mine] sc1\n\t"
        "s_nop 1"
        : [left] "=&s"(left), [cnt] "=&s"(cnt)
        : [zero] "v"(zero), [t4] "v"(t4), [t16] "v"(t16), [tagA] "s"(tagA), [tagB] "s"(tagB), [mine] "s"(mine),
          [c_done] "n"((NWAVES * WLDS + C_DONE) * 4), [c_fac] "n"((NWAVES * WLDS + C_FAC) * 4)
        : "memory", "scc", "v48", "v49", "v50", "v51", "v108", "v109", "v110", "v111", "v112", "v113", "v114", "v115",
          "v116", "v117", "v118", "v119", "v120", "v121", "v122", "v123", "v124");
  } else {
    const unsigned char* rbase = ws + REC_BASE + (size_t)grp * (NSPLIT * REC_STRIDE);
    unsigned char* obase = out + grp * (2 * D * 2);
    const unsigned t2 = t * 2;
    const unsigned go = end + TOFF * 16u;
    int need, fly, have, left, r0, r1, r2, r3, r4, r5, r6, ta, tb, keep;
    unsigned long long tp;
#define OWN_(L)                                                            \
    "s_cmp_lg_u32 %[have], 0\n\t"                                          \
    "s_cbranch_scc1 " L "f\n\t"                                            \
    "ds_read_b32 v124, %[zero] offset:%[c_done]\n\t"                       \
    "ds_read2st64_b32 v[116:117], %[t4] offset1:2\n\t"                     \
    "ds_read2st64_b32 v[118:119], %[t4] offset0:16 offset1:18\n\t"         \
    "ds_read2st64_b32 v[120:121], %[t4] offset0:32 offset1:34\n\t"         \
    "ds_read2st64_b32 v[122:123], %[t4] offset0:48 offset1:50\n\t"         \
    "s_waitcnt lgkmcnt(0)\n\t"                                             \
    "v_readfirstlane_b32 %[ta], v124\n\t"                                  \
    "v_pk_mul_f32 v[118:119], v[110:111], v[118:119]\n\t"                  \
    "v_pk_mul_f32 v[122:123], v[114:115], v[122:123]\n\t"                  \
    "v_pk_fma_f32 v[116:117], v[108:109], v[116:117], v[118:119]\n\t"      \
    "v_pk_fma_f32 v[120:121], v[112:113], v[120:121], v[122:123]\n\t"      \
    "s_cmp_eq_u32 %[ta], 4\n\t"                                            \
    "s_cselect_b32 %[have], 1, 0\n\t"                                      \
    "v_pk_add_f32 v[90:91], v[116:117], v[120:121]\n"                      \
    L ":\n\t"
#define NAP_ "s_sleep " STR_(NAP) "\n\t"
#define TURN_(I, A, B, C, D_, GAP)                                         \
    "s_waitcnt vmcnt(6)\n\t"                                               \
    "v_cmp_ne_u32 vcc, %[tagA], v" #B "\n\t"                               \
    "v_cmp_ne_u32 %[tp], %[tagB], v" #D_ "\n\t"                            \
    "s_bitset0_b32 %[fly], %[r" #I "]\n\t"                                 \
    "s_lshl_b32 %[ta], %[r" #I "], 1\n\t"                                  \
    "s_or_b64 vcc, vcc, %[tp]\n\t"                                         \
    "s_cbranch_scc1 1" #I "f\n\t"                                          \
    "s_set_gpr_idx_on %[ta], gpr_idx(DST)\n\t"                             \
    "s_bitset0_b32 %[need], %[r" #I "]\n\t"                                \
    "v_mov_b32 v76, v" #A "\n\t"                                           \
    "v_mov_b32 v77, v" #C "\n\t"                                           \
    "s_set_gpr_idx_off\n\t"                                                \
    "s_cmp_eq_u32 %[need], 0\n\t"                                          \
    "s_cbranch_scc1 90f\n"                                                 \
    "1" #I ":\n\t"                                                         \
    "s_flbit_i32_b32 %[tb], %[need]\n\t"                                   \
    "s_sub_u32 %[tb], 31, %[tb]\n\t"                                       \
    "s_andn2_b32 %[ta], %[need], %[fly]\n\t"                               \
    "s_ff1_i32_b32 %[ta], %[ta]\n\t"                                       \
    "s_cselect_b32 %[r" #I "], %[ta], %[tb]\n\t"                           \
    "s_bitset1_b32 %[fly], %[r" #I "]\n\t"                                 \
    "s_lshl_b32 %[ta], %[r" #I "], 12\n\t"                                 \
    "v_add_u32 v125, %[ta], %[t16]\n\t"                                    \
    "global_load_dwordx4 v[" #A ":" #D_ "], v125, %[rb] sc1\n\t"           \
    GAP
#define FMA_(S, X0, X1, F0, F1) "v_fma_f32 v90, v" #F0 ", v" #X0 ", v90\n\tv_fma_f32 v91, v" #F1 ", v" #X1 ", v91\n\t"
    asm volatile(
        "s_mov_b32 %[keep], m0\n\t"
        "ds_read_b128 v[92:95], %[zero] offset:%[c_rec]\n\t"
        "ds_read_b128 v[96:99], %[zero] offset:%[c_rec]+16\n\t"
        "ds_read_b128 v[100:103], %[zero] offset:%[c_rec]+32\n\t"
        "ds_read_b128 v[104:107], %[zero] offset:%[c_rec]+48\n\t"
        "ds_read_b128 v[108:111], %[zero] offset:%[c_fac]\n\t"
        "ds_read_b128 v[112:115], %[zero] offset:%[c_fac]+16\n\t"
        "v_mov_b32 v49, 0\n\tv_mov_b32 v51, 0\n\tv_mov_b32 v53, 0\n\tv_mov_b32 v55, 0\n\tv_mov_b32 v57, 0\n\t"
        "v_mov_b32 v59, 0\n\tv_mov_b32 v61, 0\n\tv_mov_b32 v63, 0\n\tv_mov_b32 v65, 0\n\tv_mov_b32 v67, 0\n\t"
        "v_mov_b32 v69, 0\n\tv_mov_b32 v71, 0\n\tv_mov_b32 v73, 0\n\tv_mov_b32 v75, 0\n\t"
        "s_movk_i32 %[need], 0x7f\n\ts_mov_b32 %[fly], 0\n\ts_mov_b32 %[have], 0\n\ts_mov_b32 %[left], 0x40000\n\t"
        "s_mov_b32 %[r0], 31\n\ts_mov_b32 %[r1], 31\n\ts_mov_b32 %[r2], 31\n\ts_mov_b32 %[r3], 31\n\t"
        "s_mov_b32 %[r4], 31\n\ts_mov_b32 %[r5], 31\n\ts_mov_b32 %[r6], 31\n"
        "1:\n\t"
        "s_sleep 1\n\t"
        "s_memrealtime %[tp]\n\t"
        "s_waitcnt lgkmcnt(0)\n\t"
        "s_lshl_b64 %[tp], %[tp], 40\n\t"
        "s_lshr_b64 %[tp], %[tp], 32\n\t"
        "s_mov_b64 vcc, %[tp]\n\t"
        "s_sub_u32 %[ta], vcc_lo, %[go]\n\t"
        "s_cmp_lt_i32 %[ta], 0\n\t"
        "s_cbranch_scc1 1b\n"
        "2:\n\t"
        TURN_(0, 48, 49, 50, 51, NAP_)
        TURN_(1, 52, 53, 54, 55, NAP_)
        TURN_(2, 56, 57, 58, 59, OWN_("3"))
        TURN_(3, 60, 61, 62, 63, NAP_)
        TURN_(4, 64, 65, 66, 67, NAP_)
        TURN_(5, 68, 69, 70, 71, OWN_("4"))
        TURN_(6, 72, 73, 74, 75, NAP_)
        "s_sub_u32 %[left], %[left], 1\n\t"
        "s_cbranch_scc0 2b\n"
        "90:\n\t"
        "s_mov_b32 %[left], 0x100000\n"
        "91:\n\t"
        OWN_("5")
        "s_cmp_lg_u32 %[have], 0\n\t"
        "s_cbranch_scc1 92f\n\t"
        "s_sub_u32 %[left], %[left], 1\n\t"
        "s_cbranch_scc0 91b\n"
        "92:\n\t"
        FMA_(0, 76, 77, 92, 93) FMA_(1, 78, 79, 94, 95) FMA_(2, 80, 81, 96, 97) FMA_(3, 82, 83, 98, 99)
        FMA_(4, 84, 85, 100, 101) FMA_(5, 86, 87, 102, 103) FMA_(6, 88, 89, 104, 105)
        "v_cvt_pk_bf16_f32 v90, v90, v91\n\t"
        "global_store_short %[t2], v90, %[ob]\n\t"
        "global_store_short_d16_hi %[t2], v90, %[ob] offset:512\n\t"
        "s_mov_b32 m0, %[keep]"
        : [need] "=&s"(need), [fly] "=&s"(fly), [have] "=&s"(have), [left] "=&s"(left), [r0] "=&s"(r0), [r1] "=&s"(r1),
          [r2] "=&s"(r2), [r3] "=&s"(r3), [r4] "=&s"(r4), [r5] "=&s"(r5), [r6] "=&s"(r6), [ta] "=&s"(ta), [tb] "=&s"(tb),
          [tp] "=&s"(tp), [keep] "=&s"(keep)
        : [zero] "v"(zero), [t2] "v"(t2), [t4] "v"(t4), [t16] "v"(t16), [tagA] "s"(tagA), [tagB] "s"(tagB), [go] "s"(go),
          [rb] "s"(rbase), [ob] "s"(obase), [c_done] "n"((NWAVES * WLDS + C_DONE) * 4),
          [c_rec] "n"((NWAVES * WLDS + C_REC) * 4), [c_fac] "n"((NWAVES * WLDS + C_FAC) * 4)
        : "memory", "scc", "vcc",
          "v48", "v49", "v50", "v51", "v52", "v53", "v54", "v55", "v56", "v57", "v58", "v59", "v60", "v61", "v62", "v63",
          "v64", "v65", "v66", "v67", "v68", "v69", "v70", "v71", "v72", "v73", "v74", "v75", "v76", "v77", "v78", "v79",
          "v80", "v81", "v82", "v83", "v84", "v85", "v86", "v87", "v88", "v89", "v90", "v91", "v92", "v93", "v94", "v95",
          "v96", "v97", "v98", "v99", "v100", "v101", "v102", "v103", "v104", "v105", "v106", "v107", "v108", "v109",
          "v110", "v111", "v112", "v113", "v114", "v115", "v116", "v117", "v118", "v119", "v120", "v121", "v122", "v123",
          "v124", "v125");
  }
  return;
  }

  {
    const unsigned char* kb = kp + base;
    const unsigned char* vb = vp + base;
    u32x4 r[2][TL];
    u32x4 q[2];
    asm volatile("global_load_dwordx4 %0, %2, %3\n\t"
                 "global_load_dwordx4 %1, %2, %3 offset:512"
                 : "=&v"(q[0]), "=&v"(q[1]) : "v"(qoff), "s"(qp) : "memory");
    issue(r[0], kb, voff);
    issue(r[1], kb + ROWB, voff);
    const unsigned late = clock256() - start;
    unsigned due = start + late + PACE0 * 16u + stag;
    auto step_for = [](unsigned span, int n) -> int {
      const int s = (int)span / n;
      return s < (PACE - 100) * 16 ? (PACE - 100) * 16 : s > (PACE + 100) * 16 ? (PACE + 100) * 16 : s;
    };
    int step = step_for((TEND - PACE0) * 16u - late, 2 * NI - 3);
    auto pace = [&]() {
      while ((int)(clock256() - due) < 0) __builtin_amdgcn_s_sleep(1);
      due += step;
    };
    asm volatile("s_barrier" ::: "memory");
    asm volatile("s_waitcnt vmcnt(16)" : "+v"(q[0]), "+v"(q[1]) :: "memory");
#define TLINE()
    float* sc = lds + wave * WLDS;
    const int h = lane >> 5;

    float mx = -INFINITY;
#pragma unroll 1
    for (int it = 0; it < NI; it += 2) {
      const unsigned char* nxt = (it + 2 < NI) ? kb + (size_t)(it + 2) * ROWB : vb;
      if (SYNC && it == IT_SYNC) {
        const unsigned end = flag_get(cw + C_END);
        if (end) step = step_for(end - due, 2 * NI - 3 - IT_SYNC);
      }
#pragma unroll
      for (int s = 0; s < 2; ++s) {
        float x[16];
        arrive(r[s]);
        TLINE();
#pragma unroll
        for (int j = 0; j < TL; ++j)
#pragma unroll
          for (int g = 0; g < 2; ++g) {
            float d = 0.f;
#pragma unroll
            for (int k = 0; k < 4; ++k) d = dot2(r[s][j][k], q[g][k], d);
            x[j * 2 + g] = d;
          }
#define TR_STEP(HALF_, CTRL, BIT)                                       \
  _Pragma("unroll") for (int i = 0; i < (HALF_); ++i) {                 \
    const float lo_ = x[i], hi_ = x[i + (HALF_)];                       \
    const float t1_ = lo_ + dpp<CTRL>(lo_), t2_ = hi_ + dpp<CTRL>(hi_); \
    x[i] = (lane & (BIT)) ? t2_ : t1_;                                  \
  }
        TR_STEP(8, XOR8, 8)
        TR_STEP(4, XOR7, 4)
        TR_STEP(2, XOR2, 2)
        TR_STEP(1, XOR1, 1)
#undef TR_STEP
        float sv = swap16_sum(x[0]);
        mx = fmaxf(mx, sv);
        if ((lane & 16) == 0) sc[(it + s) * 32 + h * 16 + (lane & 15)] = sv;
        asm volatile("" : "+v"(mx));
        pace();
        issue(r[s], nxt + s * ROWB, voff);
      }
    }

    mx = fmaxf(mx, dpp<XOR2>(mx));
    mx = fmaxf(mx, dpp<ROR4>(mx));
    mx = fmaxf(mx, dpp<XOR8>(mx));
    mx = swap32_max(mx);
    const float m0 = lane_of(mx, 0) * C2, m1 = lane_of(mx, 1) * C2;
    float l0 = 0.f, l1 = 0.f;
#pragma unroll
    for (int i = 0; i < 4; ++i) {
      f32x4 t = *reinterpret_cast<f32x4*>(sc + i * 256 + lane * 4);
      t[0] = __builtin_amdgcn_exp2f(fmaf(t[0], C2, -m0));
      t[1] = __builtin_amdgcn_exp2f(fmaf(t[1], C2, -m1));
      t[2] = __builtin_amdgcn_exp2f(fmaf(t[2], C2, -m0));
      t[3] = __builtin_amdgcn_exp2f(fmaf(t[3], C2, -m1));
      l0 += t[0] + t[2];
      l1 += t[1] + t[3];
      *reinterpret_cast<f32x4*>(sc + i * 256 + lane * 4) = t;
    }
    l0 = wave_sum(l0);
    l1 = wave_sum(l1);
    if (lane == 0) *reinterpret_cast<f32x4*>(lds + NWAVES * WLDS + C_MLV + wave * 4) = f32x4{m0, l0, m1, l1};
    flag_bump(cw + C_ML);

    f32x2 a0[4], a1[4];
#pragma unroll
    for (int k = 0; k < 4; ++k) { a0[k] = f32x2{0.f, 0.f}; a1[k] = f32x2{0.f, 0.f}; }
#pragma unroll 1
    for (int it2 = 0;; it2 += 2) {
      const int more = NI - 2 - it2;
#pragma unroll
      for (int s = 0; s < 2; ++s) {
        const int it = it2 + s;
        const float* pp = sc + it * 32 + h * 16;
        f32x4 p[TL / 2];
#pragma unroll
        for (int j = 0; j < TL / 2; ++j) p[j] = *reinterpret_cast<const f32x4*>(pp + j * 4);
        if (s == 0) arrive(r[s]);
        static_for<TL>([&](auto jI) {
          constexpr int j = decltype(jI)::value;
          if (s == 1) {
            if (PROG) arrive_one<j>(r[s][j], more);
            else if (j == 0) static_for<TL>([&](auto iI) { arrive_one<7>(r[s][decltype(iI)::value], more); });
          }
#pragma unroll
          for (int k = 0; k < 4; ++k) {
            const unsigned w = r[s][j][k];
            const f32x2 v = f32x2{__uint_as_float(w << 16), __uint_as_float(w & 0xffff0000u)};
            const float p0 = p[j >> 1][(j & 1) * 2], p1 = p[j >> 1][(j & 1) * 2 + 1];
            a0[k] = __builtin_elementwise_fma(v, f32x2{p0, p0}, a0[k]);
            a1[k] = __builtin_elementwise_fma(v, f32x2{p1, p1}, a1[k]);
          }
          if (s == 1 || j == TL - 1)
            asm volatile("" : "+v"(a0[0]), "+v"(a0[1]), "+v"(a0[2]), "+v"(a0[3]), "+v"(a1[0]), "+v"(a1[1]), "+v"(a1[2]), "+v"(a1[3]));
        });
        TLINE();
        if (s == 1 && !more) goto done;
        if (more) pace();
        if (s == 0) issue_if(r[s], vb + (size_t)(it + 2) * ROWB, voff, more);
        else issue(r[s], vb + (size_t)(it + 2) * ROWB, voff);
      }
    }
  done:
    float o[8];
#pragma unroll
    for (int k = 0; k < 8; ++k) {
      auto t = __builtin_amdgcn_permlane32_swap(__float_as_uint(a0[k >> 1][k & 1]), __float_as_uint(a1[k >> 1][k & 1]), false, false);
      o[k] = __uint_as_float(t[0]) + __uint_as_float(t[1]);
    }
    *reinterpret_cast<f32x4*>(sc + lane * 4) = f32x4{o[0], o[1], o[2], o[3]};
    *reinterpret_cast<f32x4*>(sc + lane * 4 + 272) = f32x4{o[4], o[5], o[6], o[7]};
    flag_bump(cw + C_DONE);
  }

}

}

extern "C" void kernel_launch(void* const* d_in, const int* in_sizes, int n_in, void* d_out, int out_size, void* d_ws,
                              size_t ws_size, hipStream_t stream) {
  gqa_decode<<<NGROUP * NSPLIT, NTHR, 0, stream>>>((const unsigned char*)d_in[0], (const unsigned char*)d_in[1],
                                                   (const unsigned char*)d_in[2], (unsigned char*)d_ws,
                                                   (unsigned char*)d_out);
}
```

```cpp
#define TEND2 108500
#define TEND 110600
#include <hip/hip_runtime.h>
#include <hip/hip_bf16.h>
#include <stdint.h>

#define PACE 1740
#define PACE0 2400
#define TOFF 2300
#define TARR 1300
#define GAP 2
#define NAP 0
#define ROTM 1
#define ROTV 0
#define SYNC 1
#define TOUCH 1

namespace {

constexpr int B = 4, HQ = 16, HKV = 8, G = HQ / HKV, SK = 16384, D = 256;
constexpr int NGROUP = B * HKV;
constexpr int NSPLIT = 8, NWAVES = 4, NTHR = (NWAVES + G) * 64;
constexpr int TL = 8;
constexpr int NI = SK / (NSPLIT * NWAVES * 2 * TL);
constexpr unsigned ROWB = NWAVES * TL * 1024;
constexpr int IT_SYNC = 8;
constexpr int SPIN = 1 << 24;
static_assert(G == 2, "layout assumes 2 query heads per kv head");

constexpr unsigned CTL_STRIDE = 128, CLK_BASE = 4096, ML_BASE = 8192, ML_STRIDE = 256, REC_BASE = 65536, REC_STRIDE = 2048;

typedef unsigned u32x4 __attribute__((ext_vector_type(4)));
typedef float f32x4 __attribute__((ext_vector_type(4)));
typedef float f32x2 __attribute__((ext_vector_type(2)));
typedef __bf16 bf16x2 __attribute__((ext_vector_type(2)));
typedef __attribute__((address_space(3))) int lds_int;

__device__ __forceinline__ void flag_set(lds_int* p, int v) { __hip_atomic_store(p, v, __ATOMIC_RELAXED, __HIP_MEMORY_SCOPE_WORKGROUP); }
__device__ __forceinline__ int flag_get(lds_int* p) {
  return __builtin_amdgcn_readfirstlane(__hip_atomic_load(p, __ATOMIC_RELAXED, __HIP_MEMORY_SCOPE_WORKGROUP));
}
__device__ __forceinline__ void flag_bump(lds_int* p) {
  const int one = 1;
  asm volatile("s_mov_b64 exec, 1\n\tds_add_u32 %0, %1\n\ts_mov_b64 exec, -1" :: "v"(p), "v"(one) : "memory");
}
__device__ __forceinline__ unsigned clock256() { return (unsigned)__builtin_amdgcn_s_memrealtime() * 256u; }
__device__ __forceinline__ float dot2(unsigned a, unsigned b, float acc) {
  return __builtin_amdgcn_fdot2_f32_bf16(__builtin_bit_cast(bf16x2, a), __builtin_bit_cast(bf16x2, b), acc, false);
}
template <int CTRL> __device__ __forceinline__ float dpp(float x) {
  return __builtin_bit_cast(float, __builtin_amdgcn_mov_dpp(__builtin_bit_cast(int, x), CTRL, 0xf, 0xf, true));
}
constexpr int XOR1 = 0xB1, XOR2 = 0x4E, XOR7 = 0x141, XOR8 = 0x128, ROR4 = 0x124, BCAST = 0x150;

__device__ __forceinline__ float swap16_sum(float x) {
  auto r = __builtin_amdgcn_permlane16_swap(__float_as_uint(x), __float_as_uint(x), false, false);
  return __uint_as_float(r[0]) + __uint_as_float(r[1]);
}
__device__ __forceinline__ float swap32_sum(float x) {
  auto r = __builtin_amdgcn_permlane32_swap(__float_as_uint(x), __float_as_uint(x), false, false);
  return __uint_as_float(r[0]) + __uint_as_float(r[1]);
}
__device__ __forceinline__ float swap32_max(float x) {
  auto r = __builtin_amdgcn_permlane32_swap(__float_as_uint(x), __float_as_uint(x), false, false);
  return fmaxf(__uint_as_float(r[0]), __uint_as_float(r[1]));
}
__device__ __forceinline__ float wave_sum(float x) {
  x += dpp<XOR1>(x); x += dpp<XOR2>(x); x += dpp<ROR4>(x); x += dpp<XOR8>(x);
  return swap32_sum(swap16_sum(x));
}
__device__ __forceinline__ float lane_of(float x, int l) { return __int_as_float(__builtin_amdgcn_readlane(__float_as_int(x), l)); }

#define R8_(C, r) C(r[0]), C(r[1]), C(r[2]), C(r[3]), C(r[4]), C(r[5]), C(r[6]), C(r[7])
#define LD_(R, OFF) "global_load_dwordx4 %" #R ", %[vo], %[sb] offset:" #OFF " nt\n\t"
#define LD8_ LD_(0, -4096) LD_(1, -3072) LD_(2, -2048) LD_(3, -1024) LD_(4, 0) LD_(5, 1024) LD_(6, 2048) LD_(7, 3072)
__device__ __forceinline__ void issue(u32x4 (&r)[TL], const unsigned char* base, unsigned voff) {
  asm volatile(LD8_ : R8_("=&v", r) : [vo] "v"(voff), [sb] "s"(base) : "memory");
}
__device__ __forceinline__ void issue_if(u32x4 (&r)[TL], const unsigned char* base, unsigned voff, int go) {
  asm volatile("s_cmp_eq_u32 %[go], 0\n\ts_cbranch_scc1 1f\n\t" LD8_ "1:"
               : R8_("+v", r) : [vo] "v"(voff), [sb] "s"(base), [go] "s"(go) : "memory", "scc");
}
__device__ __forceinline__ void arrive(u32x4 (&r)[TL]) { asm volatile("s_waitcnt vmcnt(8)" : R8_("+v", r) :: "memory"); }
template <int J> __device__ __forceinline__ void arrive_one(u32x4& r, int more) {
  asm volatile("s_waitcnt vmcnt(%2)\n\ts_cmp_lg_u32 %1, 0\n\ts_cbranch_scc1 1f\n\ts_waitcnt vmcnt(%3)\n1:"
               : "+v"(r) : "s"(more), "n"(15 - J), "n"(7 - J) : "memory", "scc");
}

template <int N> struct IC { static constexpr int value = N; };
template <int N, int I = 0, class Fn> __device__ __forceinline__ void static_for(Fn&& fn) {
  if constexpr (I < N) { fn(IC<I>{}); static_for<N, I + 1>(fn); }
}

constexpr int WLDS = 1024;
constexpr int HALF2 = 288;
enum { C_ML = 0, C_DONE = 1, C_READY = 2, C_END = 3, C_TAG = 4, C_NEAR = 5, C_FAC = 8, C_MLV = 16, C_REC = 32, C_WORDS = 48 };
#define S_(x) #x
#define STR_(x) S_(x)

__global__ void __launch_bounds__(NTHR, 2)
gqa_decode(const unsigned char* __restrict__ qp, const unsigned char* __restrict__ kp,
           const unsigned char* __restrict__ vp, unsigned char* __restrict__ ws, unsigned char* __restrict__ out) {
  __shared__ __attribute__((aligned(16))) float lds[NWAVES * WLDS + C_WORDS];
  const unsigned long long start64 = __builtin_amdgcn_s_memrealtime();
  __builtin_amdgcn_sched_barrier(0);
  const int tid = threadIdx.x, lane = tid & 63;
  const int wave = __builtin_amdgcn_readfirstlane(tid >> 6);
  const int grp = (blockIdx.x & 7) * 4 + ((blockIdx.x >> 3) & 3), split = blockIdx.x >> 5;
  constexpr float C2 = 0.0625f * 1.4426950408889634f;
  lds_int* cw = (lds_int*)(lds + NWAVES * WLDS);
  const unsigned voff = lane * 16;
  const unsigned stag = blockIdx.x * PACE / 16;
  const unsigned base = (unsigned)(grp * NSPLIT + split) * ((SK / NSPLIT) * D * 2) + (wave & 3) * (TL * 1024) + 4096;
  const unsigned qoff = (lane & 31) * 16 + grp * (2 * D * 2);
  asm volatile("" :: "s"(qp), "s"(kp), "s"(vp), "s"(ws), "s"(out), "s"(base), "s"(stag), "v"(qoff), "v"(voff), "s"(wave));
  const unsigned start = (unsigned)start64 * 256u;

  if (__builtin_expect(wave >= NWAVES, 0)) {
  const int t = tid - NWAVES * 64;
  const unsigned zero = 0;
  const bool adder = split == NSPLIT - 1;
  unsigned end = start + TEND * 16u + stag;
  if (wave == NWAVES) {
    unsigned char* ctl = ws + grp * CTL_STRIDE;
    unsigned epoch;
    asm volatile("global_load_dword %0, %1, %2 sc1" : "=&v"(epoch) : "v"(zero), "s"(ctl) : "memory");
    if (lane < 8) flag_set(cw + lane, 0);
    asm volatile("s_waitcnt lgkmcnt(0)\n\ts_barrier" ::: "memory");
    if (SYNC && blockIdx.x < 64 && lane == 0) {
      const unsigned long long now = __builtin_amdgcn_s_memrealtime();
      const unsigned o = CLK_BASE + blockIdx.x * 8;
      asm volatile("global_store_dwordx2 %0, %1, %2 sc1" :: "v"(o), "v"(now), "s"(ws) : "memory");
    }
    asm volatile("s_waitcnt vmcnt(0)" : "+v"(epoch) :: "memory");
    epoch = __builtin_amdgcn_readfirstlane(epoch);
    const unsigned tagA = epoch * 0x9E3779B1u + 0x7F4A7C15u, tagB = ~tagA;
    if (SYNC) {
      while ((int)(clock256() - start) < 500 * 256) __builtin_amdgcn_s_sleep(16);
      unsigned long long c;
      const unsigned o = CLK_BASE + lane * 8;
      asm volatile("global_load_dwordx2 %0, %1, %2 sc1\n\ts_waitcnt vmcnt(0)" : "=&v"(c) : "v"(o), "s"(ws) : "memory");
      const unsigned long long d = c - start64 + 512;
      const bool ok = d < 1024;
      const float sum = wave_sum(ok ? (float)(unsigned)d : 0.f);
      const int n = __builtin_popcountll(__builtin_amdgcn_ballot_w64(ok));
      if (n >= 32) {
        end += (TEND2 - TEND) * 16u + (__builtin_amdgcn_readfirstlane((int)(sum * 256.f / (float)n)) - 512 * 256);
        if (lane == 0) flag_set(cw + C_END, end | 1);
      }
    }
    if (TOUCH) {
      unsigned junk;
      asm volatile("global_load_dword %0, %1, %2 nt\n\ts_waitcnt vmcnt(0)" : "=&v"(junk) : "v"(base), "s"(vp) : "memory");
      if (adder) asm volatile("global_load_dword %0, %1, %2 nt\n\ts_waitcnt vmcnt(0)" : "=&v"(junk) : "v"(qoff), "s"(out) : "memory");
    }
    for (int spin = 0; spin < SPIN && flag_get(cw + C_ML) < NWAVES; ++spin) __builtin_amdgcn_s_sleep(32);
    asm volatile("" ::: "memory");
    const f32x2 own = *reinterpret_cast<const f32x2*>(lds + NWAVES * WLDS + C_MLV + (lane & 7) * 2);
    float mm = fmaxf(own[0], dpp<XOR2>(own[0]));
    mm = fmaxf(mm, dpp<ROR4>(mm));
    float fac = __builtin_amdgcn_exp2f(own[0] - mm);
    float ll = fac * own[1];
    ll += dpp<XOR2>(ll);
    ll += dpp<ROR4>(ll);
    unsigned char* mlb = ws + ML_BASE + grp * ML_STRIDE;
    if (!adder) {
      if (lane < 2) {
        const u32x4 ml = {__float_as_uint(mm), tagA, __float_as_uint(ll), tagB};
        const unsigned o = split * 32 + voff;
        asm volatile("global_store_dwordx4 %0, %1, %2 sc1\n\ts_nop 1" :: "v"(o), "v"(ml), "s"(mlb) : "memory");
      }
    } else {
      u32x4 ml;
      const unsigned o = (lane & 15) * 16;
      const bool mine = (lane & 15) >= 14;
#pragma unroll 1
      for (int spin = 0; spin < SPIN; ++spin) {
        __builtin_amdgcn_s_sleep(64);
        asm volatile("global_load_dwordx4 %0, %1, %2 sc1\n\ts_waitcnt vmcnt(0)" : "=&v"(ml) : "v"(o), "s"(mlb) : "memory");
        if (__builtin_amdgcn_ballot_w64(!mine && (ml[1] != tagA || ml[3] != tagB)) == 0) break;
      }
      if (lane == 0) {
        const unsigned next = epoch + 1;
        asm volatile("global_store_dword %0, %1, %2 sc1" :: "v"(zero), "v"(next), "s"(ctl) : "memory");
      }
      mm = mine ? mm : __uint_as_float(ml[0]);
      ll = mine ? ll : __uint_as_float(ml[2]);
      float gm = fmaxf(mm, dpp<XOR2>(mm));
      gm = fmaxf(gm, dpp<ROR4>(gm));
      gm = fmaxf(gm, dpp<XOR8>(gm));
      const float w = __builtin_amdgcn_exp2f(mm - gm);
      float gl = w * ll;
      gl += dpp<XOR2>(gl);
      gl += dpp<ROR4>(gl);
      gl += dpp<XOR8>(gl);
      const float wn = w / gl;
      if (lane < 16) lds[NWAVES * WLDS + C_REC + (lane & 1) * 8 + (lane >> 1)] = wn;
      const float w14 = dpp<BCAST + 14>(wn), w15 = dpp<BCAST + 15>(wn);
      fac *= (lane & 1) ? w15 : w14;
    }
    if (lane < 8) lds[NWAVES * WLDS + C_FAC + (lane & 1) * 4 + (lane >> 1)] = fac;
    asm volatile("" ::: "memory");
    if (lane == 0) { flag_set(cw + C_TAG, tagA); flag_set(cw + C_END, end | 1); flag_set(cw + C_READY, 1); }
  } else {
    asm volatile("s_barrier" ::: "memory");
    while ((int)(clock256() - end) < -4 * PACE * 16) __builtin_amdgcn_s_sleep(64);
  }
  for (int spin = 0; spin < SPIN && flag_get(cw + C_READY) == 0; ++spin) __builtin_amdgcn_s_sleep(8);
  asm volatile("" ::: "memory");
  const unsigned tag = flag_get(cw + C_TAG);
  end = flag_get(cw + C_END);
  const unsigned hd = wave - NWAVES, t16 = t * 16;
  const unsigned part = ((hd * 32 + (lane >> 1)) * 4 + (lane & 1) * HALF2) * 4, cfac = hd * 16, crec = hd * 32;
  int left, cnt;
#define LO_ " op_sel_hi:[0,1,1]\n\t"
#define HI_ " op_sel:[1,0,0]\n\t"
#define OWN_(L, M)                                                         \
    "s_mov_b32 %[left], 0x4000000\n"                                       \
    L ":\n\t"                                                              \
    "s_sub_u32 %[left], %[left], 1\n\t"                                    \
    "s_cbranch_scc1 " M "f\n\t"                                            \
    "ds_read_b32 v104, %[zero] offset:%[c_done]\n\t"                       \
    "ds_read_b128 v[88:91], %[part]\n\t"                                   \
    "ds_read_b128 v[92:95], %[part] offset:4096\n\t"                       \
    "ds_read_b128 v[96:99], %[part] offset:8192\n\t"                       \
    "ds_read_b128 v[100:103], %[part] offset:12288\n\t"                    \
    "s_waitcnt lgkmcnt(4)\n\t"                                             \
    "v_readfirstlane_b32 %[cnt], v104\n\t"                                 \
    "s_waitcnt lgkmcnt(2)\n\t"                                             \
    "v_pk_mul_f32 v[88:89], v[84:85], v[88:89] op_sel_hi:[0,1]\n\t"        \
    "v_pk_mul_f32 v[90:91], v[84:85], v[90:91] op_sel_hi:[0,1]\n\t"        \
    "v_pk_fma_f32 v[88:89], v[84:85], v[92:93], v[88:89]" HI_              \
    "v_pk_fma_f32 v[90:91], v[84:85], v[94:95], v[90:91]" HI_              \
    "s_cmp_lg_u32 %[cnt], 4\n\t"                                           \
    "s_waitcnt lgkmcnt(0)\n\t"                                             \
    "s_cbranch_scc1 " L "b\n"                                              \
    M ":\n\t"                                                              \
    "v_pk_fma_f32 v[88:89], v[86:87], v[96:97], v[88:89]" LO_              \
    "v_pk_fma_f32 v[90:91], v[86:87], v[98:99], v[90:91]" LO_              \
    "v_pk_fma_f32 v[88:89], v[86:87], v[100:101], v[88:89]" HI_            \
    "v_pk_fma_f32 v[90:91], v[86:87], v[102:103], v[90:91]" HI_
#define OWN_REGS "v84", "v85", "v86", "v87", "v88", "v89", "v90", "v91", "v92", "v93", "v94", "v95", "v96", "v97", "v98", \
                 "v99", "v100", "v101", "v102", "v103", "v104"

  if (!adder) {
    unsigned char* mine = ws + REC_BASE + (size_t)(grp * NSPLIT + split) * REC_STRIDE;
    while ((int)(clock256() - end) < 0) __builtin_amdgcn_s_sleep(4);
    asm volatile(
        "ds_read_b128 v[84:87], %[cfac] offset:%[c_fac]\n\t"
        "v_mov_b32 v19, %[tag]\n\t"
        OWN_("1", "2")
        "v_perm_b32 v16, v88, v91, %[s1]\n\t"
        "v_perm_b32 v17, v89, v91, %[s2]\n\t"
        "v_perm_b32 v18, v90, v91, %[s3]\n\t"
        "global_store_dwordx4 %[t16], v[16:19], %[mine] sc1\n\t"
        "s_nop 1"
        : [left] "=&s"(left), [cnt] "=&s"(cnt)
        : [zero] "v"(zero), [part] "v"(part), [cfac] "v"(cfac), [t16] "v"(t16), [tag] "s"(tag), [mine] "s"(mine),
          [s1] "s"(0x07060501), [s2] "s"(0x07060502), [s3] "s"(0x07060503),
          [c_done] "n"((NWAVES * WLDS + C_DONE) * 4), [c_fac] "n"((NWAVES * WLDS + C_FAC) * 4)
        : "memory", "scc", "v16", "v17", "v18", "v19", OWN_REGS);
  } else {
    const unsigned char* rbase = ws + REC_BASE + (size_t)grp * (NSPLIT * REC_STRIDE) + 4096;
    unsigned char* obase = out + grp * (2 * D * 2);
    const unsigned t8 = t * 8, t16b = t16 + 4 * REC_STRIDE;
    for (int spin = 0; spin < SPIN && flag_get(cw + C_NEAR) < NWAVES; ++spin) __builtin_amdgcn_s_sleep(1);
    const unsigned near = clock256() + TARR * 16u;
    unsigned go = end + TOFF * 16u;
    if ((int)(near - go) > 0) go = near;
    int need, ta, rounds;
    unsigned long long tp;
#define GET0 "global_load_dwordx4 v[16:19], %[t16], %[rb] offset:-4096 sc1\n"
#define GET1 "global_load_dwordx4 v[20:23], %[t16], %[rb] offset:-2048 sc1\n"
#define GET2 "global_load_dwordx4 v[24:27], %[t16], %[rb] sc1\n"
#define GET3 "global_load_dwordx4 v[28:31], %[t16], %[rb] offset:2048 sc1\n"
#define GET4 "global_load_dwordx4 v[32:35], %[t16b], %[rb] offset:-4096 sc1\n"
#define GET5 "global_load_dwordx4 v[36:39], %[t16b], %[rb] offset:-2048 sc1\n"
#define GET6 "global_load_dwordx4 v[40:43], %[t16b], %[rb] sc1\n"
#define GAP_ "\ts_sleep " STR_(GAP) "\n\t"
#define ACC_(A, B, C, W, P0, P1, SEL)                                                    \
    "v_perm_b32 v105, v" #B ", v" #A ", %[u1]\n\t"                                       \
    "v_pk_fma_f32 v[44:45], v[" #P0 ":" #P1 "], v[" #A ":" #B "], v[44:45]" SEL          \
    "v_perm_b32 v105, v" #C ", v105, %[u2]\n\t"                                          \
    "v_fma_f32 v46, v" #W ", v" #C ", v46\n\t"                                           \
    "v_fma_f32 v47, v" #W ", v105, v47\n\t"
#define ACC0 ACC_(16, 17, 18, 76, 76, 77, LO_)
#define ACC1 ACC_(20, 21, 22, 77, 76, 77, HI_)
#define ACC2 ACC_(24, 25, 26, 78, 78, 79, LO_)
#define ACC3 ACC_(28, 29, 30, 79, 78, 79, HI_)
#define ACC4 ACC_(32, 33, 34, 80, 80, 81, LO_)
#define ACC5 ACC_(36, 37, 38, 81, 80, 81, HI_)
#define ACC6 ACC_(40, 41, 42, 82, 82, 83, LO_)
#define TAKE_(R, N, T, BIT)                                                \
    "s_movk_i32 %[need], " #BIT "\n\t"                                     \
    "s_waitcnt vmcnt(" #N ")\n\t"                                          \
    MARK_##R                                                               \
    "v_cmp_ne_u32 vcc, %[tag], v" #T "\n\t"                                \
    ACC##R                                                                 \
    "s_cbranch_vccnz 80f\n\t"
#define AGAIN_(R) "s_bitcmp1_b32 %[need], " #R "\n\ts_cbranch_scc0 6" #R "f\n\t" GET##R "6" #R ":\n\t"
#define LOOK_(T, BIT)                                                      \
    "v_cmp_ne_u32 vcc, %[tag], v" #T "\n\t"                                \
    "s_cmp_lg_u64 vcc, 0\n\t"                                              \
    "s_cselect_b32 %[ta], " #BIT ", 0\n\t"                                 \
    "s_or_b32 %[need], %[need], %[ta]\n\t"
#define ZERO_ "v_mov_b32 v44, 0\n\tv_mov_b32 v45, 0\n\tv_mov_b32 v46, 0\n\tv_mov_b32 v47, 0\n\t"
#define MARK_0
#define MARK_6
#define MARK_1
#define MARK_2
#define MARK_3
#define MARK_4
#define MARK_5
    asm volatile(
        "ds_read_b128 v[76:79], %[crec] offset:%[c_rec]\n\t"
        "ds_read_b128 v[80:83], %[crec] offset:%[c_rec]+16\n\t"
        "ds_read_b128 v[84:87], %[cfac] offset:%[c_fac]\n\t"
        ZERO_
        "s_mov_b32 %[rounds], 0\n"
        "1:\n\t"
        "s_sleep 1\n\t"
        "s_memrealtime %[tp]\n\t"
        "s_waitcnt lgkmcnt(0)\n\t"
        "s_lshl_b64 %[tp], %[tp], 40\n\t"
        "s_lshr_b64 %[tp], %[tp], 32\n\t"
        "s_mov_b64 vcc, %[tp]\n\t"
        "s_sub_u32 %[ta], vcc_lo, %[go]\n\t"
        "s_cmp_lt_i32 %[ta], 0\n\t"
        "s_cbranch_scc1 1b\n\t"
        GET0 GAP_ GET1 GAP_ GET2 GAP_ GET3 GAP_ GET4 GAP_ GET5 GAP_ GET6 "\t"
        TAKE_(0, 6, 19, 1) TAKE_(1, 5, 23, 2) TAKE_(2, 4, 27, 4) TAKE_(3, 3, 31, 8) TAKE_(4, 2, 35, 16)
        OWN_("2", "3")
        TAKE_(5, 1, 39, 32) TAKE_(6, 0, 43, 64)
        "70:\n\t"
        "v_pk_add_f32 v[44:45], v[44:45], v[88:89]\n\t"
        "v_pk_add_f32 v[46:47], v[46:47], v[90:91]\n\t"
        "v_cvt_pk_bf16_f32 v44, v44, v45\n\t"
        "v_cvt_pk_bf16_f32 v45, v46, v47\n\t"
        "global_store_dwordx2 %[t8], v[44:45], %[ob]\n\t"
        "s_branch 99f\n"
        "80:\n\t"
        "s_sleep " STR_(NAP) "\n\t"
        AGAIN_(0) AGAIN_(1) AGAIN_(2) AGAIN_(3) AGAIN_(4) AGAIN_(5) AGAIN_(6)
        "s_mov_b32 %[need], 0\n\t"
        "s_add_u32 %[rounds], %[rounds], 1\n\t"
        "s_waitcnt vmcnt(0)\n\t"
        LOOK_(19, 1) LOOK_(23, 2) LOOK_(27, 4) LOOK_(31, 8) LOOK_(35, 16) LOOK_(39, 32) LOOK_(43, 64)
        "s_cbranch_scc0 82f\n\t"
        "s_cmp_lt_u32 %[rounds], 0x800000\n\t"
        "s_cbranch_scc1 80b\n"
        "82:\n\t"
        ZERO_ ACC0 ACC1 ACC2 ACC3 ACC4 ACC5 ACC6
        OWN_("4", "5")
        "s_branch 70b\n"
        "99:"
        : [need] "=&s"(need), [left] "=&s"(left), [cnt] "=&s"(cnt), [ta] "=&s"(ta), [rounds] "=&s"(rounds), [tp] "=&s"(tp)
        : [zero] "v"(zero), [t8] "v"(t8), [part] "v"(part), [t16] "v"(t16), [t16b] "v"(t16b), [cfac] "v"(cfac), [crec] "v"(crec),
          [tag] "s"(tag), [go] "s"(go), [rb] "s"(rbase), [ob] "s"(obase), [u1] "s"(0x0c04000c), [u2] "s"(0x0402010c),
          [c_done] "n"((NWAVES * WLDS + C_DONE) * 4), [c_rec] "n"((NWAVES * WLDS + C_REC) * 4),
          [c_fac] "n"((NWAVES * WLDS + C_FAC) * 4)
        : "memory", "scc", "vcc",
          "v16", "v17", "v18", "v19", "v20", "v21", "v22", "v23", "v24", "v25", "v26", "v27", "v28", "v29", "v30", "v31",
          "v32", "v33", "v34", "v35", "v36", "v37", "v38", "v39", "v40", "v41", "v42", "v43", "v44", "v45", "v46", "v47",
          "v76", "v77", "v78", "v79", "v80", "v81", "v82", "v83", "v105", OWN_REGS);
  }
  return;
  }

  {
    const unsigned char* kb = kp + base;
    const unsigned char* vb = vp + base;
    u32x4 r[2][TL];
    u32x4 q[2];
    asm volatile("global_load_dwordx4 %0, %2, %3\n\t"
                 "global_load_dwordx4 %1, %2, %3 offset:512"
                 : "=&v"(q[0]), "=&v"(q[1]) : "v"(qoff), "s"(qp) : "memory");
    auto row = [&](int n) -> unsigned { return (unsigned)((n + blockIdx.x * ROTM) & (NI - 1)) * ROWB; };
    issue(r[0], kb + row(0), voff);
    issue(r[1], kb + row(1), voff);
    const unsigned late = clock256() - start;
    unsigned due = start + late + PACE0 * 16u + stag;
    auto step_for = [](unsigned span, int n) -> int {
      const int s = (int)span / n;
      return s < (PACE - 100) * 16 ? (PACE - 100) * 16 : s > (PACE + 100) * 16 ? (PACE + 100) * 16 : s;
    };
    int step = step_for((TEND - PACE0) * 16u - late, 2 * NI - 3);
    auto pace = [&]() {
      while ((int)(clock256() - due) < 0) __builtin_amdgcn_s_sleep(1);
      due += step;
    };
    asm volatile("s_barrier" ::: "memory");
    asm volatile("s_waitcnt vmcnt(16)" : "+v"(q[0]), "+v"(q[1]) :: "memory");
#define TLINE()
    float* sc = lds + wave * WLDS;
    const int h = lane >> 5;

    float mx = -INFINITY;
#pragma unroll 1
    for (int it = 0; it < NI; it += 2) {
      const unsigned char* nxt = (it + 2 < NI) ? kb : vb;
      if (SYNC && it == IT_SYNC) {
        const unsigned end = flag_get(cw + C_END);
        if (end) step = step_for(end - due, 2 * NI - 3 - IT_SYNC);
      }
#pragma unroll
      for (int s = 0; s < 2; ++s) {
        float x[16];
        arrive(r[s]);
        TLINE();
#pragma unroll
        for (int j = 0; j < TL; ++j)
#pragma unroll
          for (int g = 0; g < 2; ++g) {
            float d = 0.f;
#pragma unroll
            for (int k = 0; k < 4; ++k) d = dot2(r[s][j][k], q[g][k], d);
            x[j * 2 + g] = d;
          }
#define TR_STEP(HALF_, CTRL, BIT)                                       \
  _Pragma("unroll") for (int i = 0; i < (HALF_); ++i) {                 \
    const float lo_ = x[i], hi_ = x[i + (HALF_)];                       \
    const float t1_ = lo_ + dpp<CTRL>(lo_), t2_ = hi_ + dpp<CTRL>(hi_); \
    x[i] = (lane & (BIT)) ? t2_ : t1_;                                  \
  }
        TR_STEP(8, XOR8, 8)
        TR_STEP(4, XOR7, 4)
        TR_STEP(2, XOR2, 2)
        TR_STEP(1, XOR1, 1)
#undef TR_STEP
        float sv = swap16_sum(x[0]);
        mx = fmaxf(mx, sv);
        if ((lane & 16) == 0) sc[(it + s) * 32 + h * 16 + (lane & 15)] = sv;
        asm volatile("" : "+v"(mx));
        pace();
        issue(r[s], nxt + row(it + 2 + s), voff);
      }
    }

    mx = fmaxf(mx, dpp<XOR2>(mx));
    mx = fmaxf(mx, dpp<ROR4>(mx));
    mx = fmaxf(mx, dpp<XOR8>(mx));
    mx = swap32_max(mx);
    const float m0 = lane_of(mx, 0) * C2, m1 = lane_of(mx, 1) * C2;
    float l0 = 0.f, l1 = 0.f;
#pragma unroll
    for (int i = 0; i < 4; ++i) {
      f32x4 t = *reinterpret_cast<f32x4*>(sc + i * 256 + lane * 4);
      t[0] = __builtin_amdgcn_exp2f(fmaf(t[0], C2, -m0));
      t[1] = __builtin_amdgcn_exp2f(fmaf(t[1], C2, -m1));
      t[2] = __builtin_amdgcn_exp2f(fmaf(t[2], C2, -m0));
      t[3] = __builtin_amdgcn_exp2f(fmaf(t[3], C2, -m1));
      l0 += t[0] + t[2];
      l1 += t[1] + t[3];
      *reinterpret_cast<f32x4*>(sc + i * 256 + lane * 4) = t;
    }
    l0 = wave_sum(l0);
    l1 = wave_sum(l1);
    if (lane == 0) *reinterpret_cast<f32x4*>(lds + NWAVES * WLDS + C_MLV + wave * 4) = f32x4{m0, l0, m1, l1};
    flag_bump(cw + C_ML);

    f32x2 a0[4], a1[4];
#pragma unroll
    for (int k = 0; k < 4; ++k) { a0[k] = f32x2{0.f, 0.f}; a1[k] = f32x2{0.f, 0.f}; }
#pragma unroll 1
    for (int it2 = 0;; it2 += 2) {
      const int more = NI - 2 - it2;
#pragma unroll
      for (int s = 0; s < 2; ++s) {
        const int it = it2 + s;
        const float* pp = sc + it * 32 + h * 16;
        f32x4 p[TL / 2];
#pragma unroll
        for (int j = 0; j < TL / 2; ++j) p[j] = *reinterpret_cast<const f32x4*>(pp + j * 4);
        if (s == 0) {
          arrive(r[s]);
          if (!more) flag_bump(cw + C_NEAR);
        }
        static_for<TL>([&](auto jI) {
          constexpr int j = decltype(jI)::value;
          if (s == 1) {
            arrive_one<j>(r[s][j], more);
          }
#pragma unroll
          for (int k = 0; k < 4; ++k) {
            const unsigned w = r[s][j][k];
            const f32x2 v = f32x2{__uint_as_float(w << 16), __uint_as_float(w & 0xffff0000u)};
            const float p0 = p[j >> 1][(j & 1) * 2], p1 = p[j >> 1][(j & 1) * 2 + 1];
            a0[k] = __builtin_elementwise_fma(v, f32x2{p0, p0}, a0[k]);
            a1[k] = __builtin_elementwise_fma(v, f32x2{p1, p1}, a1[k]);
          }
          if (s == 1 || j == TL - 1)
            asm volatile("" : "+v"(a0[0]), "+v"(a0[1]), "+v"(a0[2]), "+v"(a0[3]), "+v"(a1[0]), "+v"(a1[1]), "+v"(a1[2]), "+v"(a1[3]));
        });
        TLINE();
        if (s == 1 && !more) goto done;
        if (more) pace();
        if (s == 0) issue_if(r[s], vb + row(it + 2), voff, more);
        else issue(r[s], vb + row(it + 2), voff);
      }
    }
  done:
    float o[8];
#pragma unroll
    for (int k = 0; k < 8; ++k) {
      auto t = __builtin_amdgcn_permlane32_swap(__float_as_uint(a0[k >> 1][k & 1]), __float_as_uint(a1[k >> 1][k & 1]), false, false);
      o[k] = __uint_as_float(t[0]) + __uint_as_float(t[1]);
    }
    *reinterpret_cast<f32x4*>(sc + lane * 4) = f32x4{o[0], o[1], o[2], o[3]};
    *reinterpret_cast<f32x4*>(sc + lane * 4 + HALF2) = f32x4{o[4], o[5], o[6], o[7]};
    flag_bump(cw + C_DONE);
  }

}

}

extern "C" void kernel_launch(void* const* d_in, const int* in_sizes, int n_in, void* d_out, int out_size, void* d_ws,
                              size_t ws_size, hipStream_t stream) {
  gqa_decode<<<NGROUP * NSPLIT, NTHR, 0, stream>>>((const unsigned char*)d_in[0], (const unsigned char*)d_in[1],
                                                   (const unsigned char*)d_in[2], (unsigned char*)d_ws,
                                                   (unsigned char*)d_out);
}
```

```cpp
#include <hip/hip_runtime.h>
#include <hip/hip_bf16.h>
#include <stdint.h>

#define PACE 1740
#define PACE0 2400
#define TEND 110600
#define TEND2 108500
#define TOFF 2300
#define TARR 1300
#define GAP 2
#define NAP 0
#define ROTM 1
#define SYNC 1
#define TOUCH 1

namespace {

constexpr int B = 4, HQ = 16, HKV = 8, G = HQ / HKV, SK = 16384, D = 256;
constexpr int NGROUP = B * HKV;
constexpr int NSPLIT = 8, NWAVES = 4, NTHR = (NWAVES + G) * 64;
constexpr int TL = 8;
constexpr int NI = SK / (NSPLIT * NWAVES * 2 * TL);
constexpr unsigned ROWB = NWAVES * TL * 1024;
constexpr int IT_SYNC = 8;
constexpr int SPIN = 1 << 24;
static_assert(G == 2, "layout assumes 2 query heads per kv head");

constexpr unsigned CTL_STRIDE = 128, CLK_BASE = 4096, ML_BASE = 8192, ML_STRIDE = 256, REC_BASE = 65536, REC_STRIDE = 2048;

typedef unsigned u32x4 __attribute__((ext_vector_type(4)));
typedef float f32x4 __attribute__((ext_vector_type(4)));
typedef float f32x2 __attribute__((ext_vector_type(2)));
typedef __bf16 bf16x2 __attribute__((ext_vector_type(2)));
typedef __attribute__((address_space(3))) int lds_int;

__device__ __forceinline__ void flag_set(lds_int* p, int v) { __hip_atomic_store(p, v, __ATOMIC_RELAXED, __HIP_MEMORY_SCOPE_WORKGROUP); }
__device__ __forceinline__ int flag_get(lds_int* p) {
  return __builtin_amdgcn_readfirstlane(__hip_atomic_load(p, __ATOMIC_RELAXED, __HIP_MEMORY_SCOPE_WORKGROUP));
}
__device__ __forceinline__ void flag_bump(lds_int* p) {
  const int one = 1;
  asm volatile("s_mov_b64 exec, 1\n\tds_add_u32 %0, %1\n\ts_mov_b64 exec, -1" :: "v"(p), "v"(one) : "memory");
}
__device__ __forceinline__ unsigned clock256() { return (unsigned)__builtin_amdgcn_s_memrealtime() * 256u; }
__device__ __forceinline__ float dot2(unsigned a, unsigned b, float acc) {
  return __builtin_amdgcn_fdot2_f32_bf16(__builtin_bit_cast(bf16x2, a), __builtin_bit_cast(bf16x2, b), acc, false);
}
template <int CTRL> __device__ __forceinline__ float dpp(float x) {
  return __builtin_bit_cast(float, __builtin_amdgcn_mov_dpp(__builtin_bit_cast(int, x), CTRL, 0xf, 0xf, true));
}
constexpr int XOR1 = 0xB1, XOR2 = 0x4E, XOR7 = 0x141, XOR8 = 0x128, ROR4 = 0x124, BCAST = 0x150;

__device__ __forceinline__ float swap16_sum(float x) {
  auto r = __builtin_amdgcn_permlane16_swap(__float_as_uint(x), __float_as_uint(x), false, false);
  return __uint_as_float(r[0]) + __uint_as_float(r[1]);
}
__device__ __forceinline__ float swap32_sum(float x) {
  auto r = __builtin_amdgcn_permlane32_swap(__float_as_uint(x), __float_as_uint(x), false, false);
  return __uint_as_float(r[0]) + __uint_as_float(r[1]);
}
__device__ __forceinline__ float swap32_max(float x) {
  auto r = __builtin_amdgcn_permlane32_swap(__float_as_uint(x), __float_as_uint(x), false, false);
  return fmaxf(__uint_as_float(r[0]), __uint_as_float(r[1]));
}
__device__ __forceinline__ float wave_sum(float x) {
  x += dpp<XOR1>(x); x += dpp<XOR2>(x); x += dpp<ROR4>(x); x += dpp<XOR8>(x);
  return swap32_sum(swap16_sum(x));
}
__device__ __forceinline__ float lane_of(float x, int l) { return __int_as_float(__builtin_amdgcn_readlane(__float_as_int(x), l)); }

#define R8_(C, r) C(r[0]), C(r[1]), C(r[2]), C(r[3]), C(r[4]), C(r[5]), C(r[6]), C(r[7])
#define LD_(R, OFF) "global_load_dwordx4 %" #R ", %[vo], %[sb] offset:" #OFF " nt\n\t"
#define LD8_ LD_(0, -4096) LD_(1, -3072) LD_(2, -2048) LD_(3, -1024) LD_(4, 0) LD_(5, 1024) LD_(6, 2048) LD_(7, 3072)
__device__ __forceinline__ void issue(u32x4 (&r)[TL], const unsigned char* base, unsigned voff) {
  asm volatile(LD8_ : R8_("=&v", r) : [vo] "v"(voff), [sb] "s"(base) : "memory");
}
__device__ __forceinline__ void issue_if(u32x4 (&r)[TL], const unsigned char* base, unsigned voff, int go) {
  asm volatile("s_cmp_eq_u32 %[go], 0\n\ts_cbranch_scc1 1f\n\t" LD8_ "1:"
               : R8_("+v", r) : [vo] "v"(voff), [sb] "s"(base), [go] "s"(go) : "memory", "scc");
}
__device__ __forceinline__ void arrive(u32x4 (&r)[TL]) { asm volatile("s_waitcnt vmcnt(8)" : R8_("+v", r) :: "memory"); }
template <int J> __device__ __forceinline__ void arrive_one(u32x4& r, int more) {
  asm volatile("s_waitcnt vmcnt(%2)\n\ts_cmp_lg_u32 %1, 0\n\ts_cbranch_scc1 1f\n\ts_waitcnt vmcnt(%3)\n1:"
               : "+v"(r) : "s"(more), "n"(15 - J), "n"(7 - J) : "memory", "scc");
}

template <int N> struct IC { static constexpr int value = N; };
template <int N, int I = 0, class Fn> __device__ __forceinline__ void static_for(Fn&& fn) {
  if constexpr (I < N) { fn(IC<I>{}); static_for<N, I + 1>(fn); }
}

constexpr int WLDS = 1024;
constexpr int HALF2 = 288;
enum { C_ML = 0, C_DONE = 1, C_READY = 2, C_END = 3, C_TAG = 4, C_NEAR = 5, C_FAC = 8, C_MLV = 16, C_REC = 32, C_WORDS = 48 };
#define S_(x) #x
#define STR_(x) S_(x)

__global__ void __launch_bounds__(NTHR, 2)
gqa_decode(const unsigned char* __restrict__ qp, const unsigned char* __restrict__ kp,
           const unsigned char* __restrict__ vp, unsigned char* __restrict__ ws, unsigned char* __restrict__ out) {
  __shared__ __attribute__((aligned(16))) float lds[NWAVES * WLDS + C_WORDS];
  const unsigned long long start64 = __builtin_amdgcn_s_memrealtime();
  __builtin_amdgcn_sched_barrier(0);
  const int tid = threadIdx.x, lane = tid & 63;
  const int wave = __builtin_amdgcn_readfirstlane(tid >> 6);
  const int grp = (blockIdx.x & 7) * 4 + ((blockIdx.x >> 3) & 3), split = blockIdx.x >> 5;
  constexpr float C2 = 0.0625f * 1.4426950408889634f;
  lds_int* cw = (lds_int*)(lds + NWAVES * WLDS);
  const unsigned voff = lane * 16;
  const unsigned stag = blockIdx.x * PACE / 16;
  const unsigned base = (unsigned)(grp * NSPLIT + split) * ((SK / NSPLIT) * D * 2) + (wave & 3) * (TL * 1024) + 4096;
  const unsigned qoff = (lane & 31) * 16 + grp * (2 * D * 2);
  asm volatile("" :: "s"(qp), "s"(kp), "s"(vp), "s"(ws), "s"(out), "s"(base), "s"(stag), "v"(qoff), "v"(voff), "s"(wave));
  const unsigned start = (unsigned)start64 * 256u;

  if (__builtin_expect(wave >= NWAVES, 0)) {
  const int t = tid - NWAVES * 64;
  const unsigned zero = 0;
  const bool adder = split == NSPLIT - 1;
  unsigned end = start + TEND * 16u + stag;
  if (wave == NWAVES) {
    unsigned char* ctl = ws + grp * CTL_STRIDE;
    unsigned epoch;
    asm volatile("global_load_dword %0, %1, %2 sc1" : "=&v"(epoch) : "v"(zero), "s"(ctl) : "memory");
    if (lane < 8) flag_set(cw + lane, 0);
    asm volatile("s_waitcnt lgkmcnt(0)\n\ts_barrier" ::: "memory");
    if (SYNC && blockIdx.x < 64 && lane == 0) {
      const unsigned long long now = __builtin_amdgcn_s_memrealtime();
      const unsigned o = CLK_BASE + blockIdx.x * 8;
      asm volatile("global_store_dwordx2 %0, %1, %2 sc1" :: "v"(o), "v"(now), "s"(ws) : "memory");
    }
    asm volatile("s_waitcnt vmcnt(0)" : "+v"(epoch) :: "memory");
    epoch = __builtin_amdgcn_readfirstlane(epoch);
    const unsigned tagA = epoch * 0x9E3779B1u + 0x7F4A7C15u, tagB = ~tagA;
    if (SYNC) {
      while ((int)(clock256() - start) < 500 * 256) __builtin_amdgcn_s_sleep(16);
      unsigned long long c;
      const unsigned o = CLK_BASE + lane * 8;
      asm volatile("global_load_dwordx2 %0, %1, %2 sc1\n\ts_waitcnt vmcnt(0)" : "=&v"(c) : "v"(o), "s"(ws) : "memory");
      const unsigned long long d = c - start64 + 512;
      const bool ok = d < 1024;
      const float sum = wave_sum(ok ? (float)(unsigned)d : 0.f);
      const int n = __builtin_popcountll(__builtin_amdgcn_ballot_w64(ok));
      if (n >= 32) {
        end += (TEND2 - TEND) * 16u + (__builtin_amdgcn_readfirstlane((int)(sum * 256.f / (float)n)) - 512 * 256);
        if (lane == 0) flag_set(cw + C_END, end | 1);
      }
    }
    if (TOUCH) {
      unsigned junk;
      asm volatile("global_load_dword %0, %1, %2 nt\n\ts_waitcnt vmcnt(0)" : "=&v"(junk) : "v"(base), "s"(vp) : "memory");
      if (adder) asm volatile("global_load_dword %0, %1, %2 nt\n\ts_waitcnt vmcnt(0)" : "=&v"(junk) : "v"(qoff), "s"(out) : "memory");
    }
    for (int spin = 0; spin < SPIN && flag_get(cw + C_ML) < NWAVES; ++spin) __builtin_amdgcn_s_sleep(32);
    asm volatile("" ::: "memory");
    const f32x2 own = *reinterpret_cast<const f32x2*>(lds + NWAVES * WLDS + C_MLV + (lane & 7) * 2);
    float mm = fmaxf(own[0], dpp<XOR2>(own[0]));
    mm = fmaxf(mm, dpp<ROR4>(mm));
    float fac = __builtin_amdgcn_exp2f(own[0] - mm);
    float ll = fac * own[1];
    ll += dpp<XOR2>(ll);
    ll += dpp<ROR4>(ll);
    unsigned char* mlb = ws + ML_BASE + grp * ML_STRIDE;
    if (!adder) {
      if (lane < 2) {
        const u32x4 ml = {__float_as_uint(mm), tagA, __float_as_uint(ll), tagB};
        const unsigned o = split * 32 + voff;
        asm volatile("global_store_dwordx4 %0, %1, %2 sc1\n\ts_nop 1" :: "v"(o), "v"(ml), "s"(mlb) : "memory");
      }
    } else {
      u32x4 ml;
      const unsigned o = (lane & 15) * 16;
      const bool mine = (lane & 15) >= 14;
#pragma unroll 1
      for (int spin = 0; spin < SPIN; ++spin) {
        __builtin_amdgcn_s_sleep(64);
        asm volatile("global_load_dwordx4 %0, %1, %2 sc1\n\ts_waitcnt vmcnt(0)" : "=&v"(ml) : "v"(o), "s"(mlb) : "memory");
        if (__builtin_amdgcn_ballot_w64(!mine && (ml[1] != tagA || ml[3] != tagB)) == 0) break;
      }
      if (lane == 0) {
        const unsigned next = epoch + 1;
        asm volatile("global_store_dword %0, %1, %2 sc1" :: "v"(zero), "v"(next), "s"(ctl) : "memory");
      }
      mm = mine ? mm : __uint_as_float(ml[0]);
      ll = mine ? ll : __uint_as_float(ml[2]);
      float gm = fmaxf(mm, dpp<XOR2>(mm));
      gm = fmaxf(gm, dpp<ROR4>(gm));
      gm = fmaxf(gm, dpp<XOR8>(gm));
      const float w = __builtin_amdgcn_exp2f(mm - gm);
      float gl = w * ll;
      gl += dpp<XOR2>(gl);
      gl += dpp<ROR4>(gl);
      gl += dpp<XOR8>(gl);
      const float wn = w / gl;
      if (lane < 16) lds[NWAVES * WLDS + C_REC + (lane & 1) * 8 + (lane >> 1)] = wn;
      const float w14 = dpp<BCAST + 14>(wn), w15 = dpp<BCAST + 15>(wn);
      fac *= (lane & 1) ? w15 : w14;
    }
    if (lane < 8) lds[NWAVES * WLDS + C_FAC + (lane & 1) * 4 + (lane >> 1)] = fac;
    asm volatile("" ::: "memory");
    if (lane == 0) { flag_set(cw + C_TAG, tagA); flag_set(cw + C_END, end | 1); flag_set(cw + C_READY, 1); }
  } else {
    asm volatile("s_barrier" ::: "memory");
    while ((int)(clock256() - end) < -4 * PACE * 16) __builtin_amdgcn_s_sleep(64);
  }
  for (int spin = 0; spin < SPIN && flag_get(cw + C_READY) == 0; ++spin) __builtin_amdgcn_s_sleep(8);
  asm volatile("" ::: "memory");
  const unsigned tag = flag_get(cw + C_TAG);
  end = flag_get(cw + C_END);
  const unsigned hd = wave - NWAVES, t16 = t * 16;
  const unsigned part = ((hd * 32 + (lane >> 1)) * 4 + (lane & 1) * HALF2) * 4, cfac = hd * 16, crec = hd * 32;
  int left, cnt;
#define LO_ " op_sel_hi:[0,1,1]\n\t"
#define HI_ " op_sel:[1,0,0]\n\t"
#define OWN_(L, M)                                                         \
    "s_mov_b32 %[left], 0x4000000\n"                                       \
    L ":\n\t"                                                              \
    "s_sub_u32 %[left], %[left], 1\n\t"                                    \
    "s_cbranch_scc1 " M "f\n\t"                                            \
    "ds_read_b32 v104, %[zero] offset:%[c_done]\n\t"                       \
    "ds_read_b128 v[88:91], %[part]\n\t"                                   \
    "ds_read_b128 v[92:95], %[part] offset:4096\n\t"                       \
    "ds_read_b128 v[96:99], %[part] offset:8192\n\t"                       \
    "ds_read_b128 v[100:103], %[part] offset:12288\n\t"                    \
    "s_waitcnt lgkmcnt(4)\n\t"                                             \
    "v_readfirstlane_b32 %[cnt], v104\n\t"                                 \
    "s_waitcnt lgkmcnt(2)\n\t"                                             \
    "v_pk_mul_f32 v[88:89], v[84:85], v[88:89] op_sel_hi:[0,1]\n\t"        \
    "v_pk_mul_f32 v[90:91], v[84:85], v[90:91] op_sel_hi:[0,1]\n\t"        \
    "v_pk_fma_f32 v[88:89], v[84:85], v[92:93], v[88:89]" HI_              \
    "v_pk_fma_f32 v[90:91], v[84:85], v[94:95], v[90:91]" HI_              \
    "s_cmp_lg_u32 %[cnt], 4\n\t"                                           \
    "s_waitcnt lgkmcnt(0)\n\t"                                             \
    "s_cbranch_scc1 " L "b\n"                                              \
    M ":\n\t"                                                              \
    "v_pk_fma_f32 v[88:89], v[86:87], v[96:97], v[88:89]" LO_              \
    "v_pk_fma_f32 v[90:91], v[86:87], v[98:99], v[90:91]" LO_              \
    "v_pk_fma_f32 v[88:89], v[86:87], v[100:101], v[88:89]" HI_            \
    "v_pk_fma_f32 v[90:91], v[86:87], v[102:103], v[90:91]" HI_
#define OWN_REGS "v84", "v85", "v86", "v87", "v88", "v89", "v90", "v91", "v92", "v93", "v94", "v95", "v96", "v97", "v98", \
                 "v99", "v100", "v101", "v102", "v103", "v104"

  if (!adder) {
    unsigned char* mine = ws + REC_BASE + (size_t)(grp * NSPLIT + split) * REC_STRIDE;
    while ((int)(clock256() - end) < 0) __builtin_amdgcn_s_sleep(4);
    asm volatile(
        "ds_read_b128 v[84:87], %[cfac] offset:%[c_fac]\n\t"
        "v_mov_b32 v19, %[tag]\n\t"
        OWN_("1", "2")
        "v_perm_b32 v16, v88, v91, %[s1]\n\t"
        "v_perm_b32 v17, v89, v91, %[s2]\n\t"
        "v_perm_b32 v18, v90, v91, %[s3]\n\t"
        "global_store_dwordx4 %[t16], v[16:19], %[mine] sc1\n\t"
        "s_nop 1"
        : [left] "=&s"(left), [cnt] "=&s"(cnt)
        : [zero] "v"(zero), [part] "v"(part), [cfac] "v"(cfac), [t16] "v"(t16), [tag] "s"(tag), [mine] "s"(mine),
          [s1] "s"(0x07060501), [s2] "s"(0x07060502), [s3] "s"(0x07060503),
          [c_done] "n"((NWAVES * WLDS + C_DONE) * 4), [c_fac] "n"((NWAVES * WLDS + C_FAC) * 4)
        : "memory", "scc", "v16", "v17", "v18", "v19", OWN_REGS);
  } else {
    const unsigned char* rbase = ws + REC_BASE + (size_t)grp * (NSPLIT * REC_STRIDE) + 4096;
    unsigned char* obase = out + grp * (2 * D * 2);
    const unsigned t8 = t * 8, t16b = t16 + 4 * REC_STRIDE;
    for (int spin = 0; spin < SPIN && flag_get(cw + C_NEAR) < NWAVES; ++spin) __builtin_amdgcn_s_sleep(1);
    const unsigned near = clock256() + TARR * 16u;
    unsigned go = end + TOFF * 16u;
    if ((int)(near - go) > 0) go = near;
    int need, ta, rounds;
    unsigned long long tp;
#define GET0 "global_load_dwordx4 v[16:19], %[t16], %[rb] offset:-4096 sc1\n"
#define GET1 "global_load_dwordx4 v[20:23], %[t16], %[rb] offset:-2048 sc1\n"
#define GET2 "global_load_dwordx4 v[24:27], %[t16], %[rb] sc1\n"
#define GET3 "global_load_dwordx4 v[28:31], %[t16], %[rb] offset:2048 sc1\n"
#define GET4 "global_load_dwordx4 v[32:35], %[t16b], %[rb] offset:-4096 sc1\n"
#define GET5 "global_load_dwordx4 v[36:39], %[t16b], %[rb] offset:-2048 sc1\n"
#define GET6 "global_load_dwordx4 v[40:43], %[t16b], %[rb] sc1\n"
#define GAP_ "\ts_sleep " STR_(GAP) "\n\t"
#define ACC_(A, B, C, W, P0, P1, SEL)                                                    \
    "v_perm_b32 v105, v" #B ", v" #A ", %[u1]\n\t"                                       \
    "v_pk_fma_f32 v[44:45], v[" #P0 ":" #P1 "], v[" #A ":" #B "], v[44:45]" SEL          \
    "v_perm_b32 v105, v" #C ", v105, %[u2]\n\t"                                          \
    "v_fma_f32 v46, v" #W ", v" #C ", v46\n\t"                                           \
    "v_fma_f32 v47, v" #W ", v105, v47\n\t"
#define ACC0 ACC_(16, 17, 18, 76, 76, 77, LO_)
#define ACC1 ACC_(20, 21, 22, 77, 76, 77, HI_)
#define ACC2 ACC_(24, 25, 26, 78, 78, 79, LO_)
#define ACC3 ACC_(28, 29, 30, 79, 78, 79, HI_)
#define ACC4 ACC_(32, 33, 34, 80, 80, 81, LO_)
#define ACC5 ACC_(36, 37, 38, 81, 80, 81, HI_)
#define ACC6 ACC_(40, 41, 42, 82, 82, 83, LO_)
#define TAKE_(R, N, T, BIT)                                                \
    "s_movk_i32 %[need], " #BIT "\n\t"                                     \
    "s_waitcnt vmcnt(" #N ")\n\t"                                          \
    "v_cmp_ne_u32 vcc, %[tag], v" #T "\n\t"                                \
    ACC##R                                                                 \
    "s_cbranch_vccnz 80f\n\t"
#define AGAIN_(R) "s_bitcmp1_b32 %[need], " #R "\n\ts_cbranch_scc0 6" #R "f\n\t" GET##R "6" #R ":\n\t"
#define LOOK_(T, BIT)                                                      \
    "v_cmp_ne_u32 vcc, %[tag], v" #T "\n\t"                                \
    "s_cmp_lg_u64 vcc, 0\n\t"                                              \
    "s_cselect_b32 %[ta], " #BIT ", 0\n\t"                                 \
    "s_or_b32 %[need], %[need], %[ta]\n\t"
#define ZERO_ "v_mov_b32 v44, 0\n\tv_mov_b32 v45, 0\n\tv_mov_b32 v46, 0\n\tv_mov_b32 v47, 0\n\t"
    asm volatile(
        "ds_read_b128 v[76:79], %[crec] offset:%[c_rec]\n\t"
        "ds_read_b128 v[80:83], %[crec] offset:%[c_rec]+16\n\t"
        "ds_read_b128 v[84:87], %[cfac] offset:%[c_fac]\n\t"
        ZERO_
        "s_mov_b32 %[rounds], 0\n"
        "1:\n\t"
        "s_sleep 1\n\t"
        "s_memrealtime %[tp]\n\t"
        "s_waitcnt lgkmcnt(0)\n\t"
        "s_lshl_b64 %[tp], %[tp], 40\n\t"
        "s_lshr_b64 %[tp], %[tp], 32\n\t"
        "s_mov_b64 vcc, %[tp]\n\t"
        "s_sub_u32 %[ta], vcc_lo, %[go]\n\t"
        "s_cmp_lt_i32 %[ta], 0\n\t"
        "s_cbranch_scc1 1b\n\t"
        GET0 GAP_ GET1 GAP_ GET2 GAP_ GET3 GAP_ GET4 GAP_ GET5 GAP_ GET6 "\t"
        TAKE_(0, 6, 19, 1) TAKE_(1, 5, 23, 2) TAKE_(2, 4, 27, 4) TAKE_(3, 3, 31, 8) TAKE_(4, 2, 35, 16)
        OWN_("2", "3")
        TAKE_(5, 1, 39, 32) TAKE_(6, 0, 43, 64)
        "70:\n\t"
        "v_pk_add_f32 v[44:45], v[44:45], v[88:89]\n\t"
        "v_pk_add_f32 v[46:47], v[46:47], v[90:91]\n\t"
        "v_cvt_pk_bf16_f32 v44, v44, v45\n\t"
        "v_cvt_pk_bf16_f32 v45, v46, v47\n\t"
        "global_store_dwordx2 %[t8], v[44:45], %[ob]\n\t"
        "s_branch 99f\n"
        "80:\n\t"
        "s_sleep " STR_(NAP) "\n\t"
        AGAIN_(0) AGAIN_(1) AGAIN_(2) AGAIN_(3) AGAIN_(4) AGAIN_(5) AGAIN_(6)
        "s_mov_b32 %[need], 0\n\t"
        "s_add_u32 %[rounds], %[rounds], 1\n\t"
        "s_waitcnt vmcnt(0)\n\t"
        LOOK_(19, 1) LOOK_(23, 2) LOOK_(27, 4) LOOK_(31, 8) LOOK_(35, 16) LOOK_(39, 32) LOOK_(43, 64)
        "s_cbranch_scc0 82f\n\t"
        "s_cmp_lt_u32 %[rounds], 0x800000\n\t"
        "s_cbranch_scc1 80b\n"
        "82:\n\t"
        ZERO_ ACC0 ACC1 ACC2 ACC3 ACC4 ACC5 ACC6
        OWN_("4", "5")
        "s_branch 70b\n"
        "99:"
        : [need] "=&s"(need), [left] "=&s"(left), [cnt] "=&s"(cnt), [ta] "=&s"(ta), [rounds] "=&s"(rounds), [tp] "=&s"(tp)
        : [zero] "v"(zero), [t8] "v"(t8), [part] "v"(part), [t16] "v"(t16), [t16b] "v"(t16b), [cfac] "v"(cfac), [crec] "v"(crec),
          [tag] "s"(tag), [go] "s"(go), [rb] "s"(rbase), [ob] "s"(obase), [u1] "s"(0x0c04000c), [u2] "s"(0x0402010c),
          [c_done] "n"((NWAVES * WLDS + C_DONE) * 4), [c_rec] "n"((NWAVES * WLDS + C_REC) * 4),
          [c_fac] "n"((NWAVES * WLDS + C_FAC) * 4)
        : "memory", "scc", "vcc",
          "v16", "v17", "v18", "v19", "v20", "v21", "v22", "v23", "v24", "v25", "v26", "v27", "v28", "v29", "v30", "v31",
          "v32", "v33", "v34", "v35", "v36", "v37", "v38", "v39", "v40", "v41", "v42", "v43", "v44", "v45", "v46", "v47",
          "v76", "v77", "v78", "v79", "v80", "v81", "v82", "v83", "v105", OWN_REGS);
  }
  return;
  }

  {
    const unsigned char* kb = kp + base;
    const unsigned char* vb = vp + base;
    u32x4 r[2][TL];
    u32x4 q[2];
    asm volatile("global_load_dwordx4 %0, %2, %3\n\t"
                 "global_load_dwordx4 %1, %2, %3 offset:512"
                 : "=&v"(q[0]), "=&v"(q[1]) : "v"(qoff), "s"(qp) : "memory");
    auto row = [&](int n) -> unsigned { return (unsigned)((n + blockIdx.x * ROTM) & (NI - 1)) * ROWB; };
    issue(r[0], kb + row(0), voff);
    issue(r[1], kb + row(1), voff);
    const unsigned late = clock256() - start;
    unsigned due = start + late + PACE0 * 16u + stag;
    auto step_for = [](unsigned span, int n) -> int {
      const int s = (int)span / n;
      return s < (PACE - 100) * 16 ? (PACE - 100) * 16 : s > (PACE + 100) * 16 ? (PACE + 100) * 16 : s;
    };
    int step = step_for((TEND - PACE0) * 16u - late, 2 * NI - 3);
    auto pace = [&]() {
      while ((int)(clock256() - due) < 0) __builtin_amdgcn_s_sleep(1);
      due += step;
    };
    asm volatile("s_barrier" ::: "memory");
    asm volatile("s_waitcnt vmcnt(16)" : "+v"(q[0]), "+v"(q[1]) :: "memory");
    float* sc = lds + wave * WLDS;
    const int h = lane >> 5;

    float mx = -INFINITY;
#pragma unroll 1
    for (int it = 0; it < NI; it += 2) {
      const unsigned char* nxt = (it + 2 < NI) ? kb : vb;
      if (SYNC && it == IT_SYNC) {
        const unsigned end = flag_get(cw + C_END);
        if (end) step = step_for(end - due, 2 * NI - 3 - IT_SYNC);
      }
#pragma unroll
      for (int s = 0; s < 2; ++s) {
        float x[16];
        arrive(r[s]);
#pragma unroll
        for (int j = 0; j < TL; ++j)
#pragma unroll
          for (int g = 0; g < 2; ++g) {
            float d = 0.f;
#pragma unroll
            for (int k = 0; k < 4; ++k) d = dot2(r[s][j][k], q[g][k], d);
            x[j * 2 + g] = d;
          }
#define TR_STEP(HALF_, CTRL, BIT)                                       \
  _Pragma("unroll") for (int i = 0; i < (HALF_); ++i) {                 \
    const float lo_ = x[i], hi_ = x[i + (HALF_)];                       \
    const float t1_ = lo_ + dpp<CTRL>(lo_), t2_ = hi_ + dpp<CTRL>(hi_); \
    x[i] = (lane & (BIT)) ? t2_ : t1_;                                  \
  }
        TR_STEP(8, XOR8, 8)
        TR_STEP(4, XOR7, 4)
        TR_STEP(2, XOR2, 2)
        TR_STEP(1, XOR1, 1)
#undef TR_STEP
        float sv = swap16_sum(x[0]);
        mx = fmaxf(mx, sv);
        if ((lane & 16) == 0) sc[(it + s) * 32 + h * 16 + (lane & 15)] = sv;
        asm volatile("" : "+v"(mx));
        pace();
        issue(r[s], nxt + row(it + 2 + s), voff);
      }
    }

    mx = fmaxf(mx, dpp<XOR2>(mx));
    mx = fmaxf(mx, dpp<ROR4>(mx));
    mx = fmaxf(mx, dpp<XOR8>(mx));
    mx = swap32_max(mx);
    const float m0 = lane_of(mx, 0) * C2, m1 = lane_of(mx, 1) * C2;
    float l0 = 0.f, l1 = 0.f;
#pragma unroll
    for (int i = 0; i < 4; ++i) {
      f32x4 t = *reinterpret_cast<f32x4*>(sc + i * 256 + lane * 4);
      t[0] = __builtin_amdgcn_exp2f(fmaf(t[0], C2, -m0));
      t[1] = __builtin_amdgcn_exp2f(fmaf(t[1], C2, -m1));
      t[2] = __builtin_amdgcn_exp2f(fmaf(t[2], C2, -m0));
      t[3] = __builtin_amdgcn_exp2f(fmaf(t[3], C2, -m1));
      l0 += t[0] + t[2];
      l1 += t[1] + t[3];
      *reinterpret_cast<f32x4*>(sc + i * 256 + lane * 4) = t;
    }
    l0 = wave_sum(l0);
    l1 = wave_sum(l1);
    if (lane == 0) *reinterpret_cast<f32x4*>(lds + NWAVES * WLDS + C_MLV + wave * 4) = f32x4{m0, l0, m1, l1};
    flag_bump(cw + C_ML);

    f32x2 a0[4], a1[4];
#pragma unroll
    for (int k = 0; k < 4; ++k) { a0[k] = f32x2{0.f, 0.f}; a1[k] = f32x2{0.f, 0.f}; }
#pragma unroll 1
    for (int it2 = 0;; it2 += 2) {
      const int more = NI - 2 - it2;
#pragma unroll
      for (int s = 0; s < 2; ++s) {
        const int it = it2 + s;
        const float* pp = sc + it * 32 + h * 16;
        f32x4 p[TL / 2];
#pragma unroll
        for (int j = 0; j < TL / 2; ++j) p[j] = *reinterpret_cast<const f32x4*>(pp + j * 4);
        if (s == 0) {
          arrive(r[s]);
          if (!more) flag_bump(cw + C_NEAR);
        }
        static_for<TL>([&](auto jI) {
          constexpr int j = decltype(jI)::value;
          if (s == 1) {
            arrive_one<j>(r[s][j], more);
          }
#pragma unroll
          for (int k = 0; k < 4; ++k) {
            const unsigned w = r[s][j][k];
            const f32x2 v = f32x2{__uint_as_float(w << 16), __uint_as_float(w & 0xffff0000u)};
            const float p0 = p[j >> 1][(j & 1) * 2], p1 = p[j >> 1][(j & 1) * 2 + 1];
            a0[k] = __builtin_elementwise_fma(v, f32x2{p0, p0}, a0[k]);
            a1[k] = __builtin_elementwise_fma(v, f32x2{p1, p1}, a1[k]);
          }
          if (s == 1 || j == TL - 1)
            asm volatile("" : "+v"(a0[0]), "+v"(a0[1]), "+v"(a0[2]), "+v"(a0[3]), "+v"(a1[0]), "+v"(a1[1]), "+v"(a1[2]), "+v"(a1[3]));
        });
        if (s == 1 && !more) goto done;
        if (more) pace();
        if (s == 0) issue_if(r[s], vb + row(it + 2), voff, more);
        else issue(r[s], vb + row(it + 2), voff);
      }
    }
  done:
    float o[8];
#pragma unroll
    for (int k = 0; k < 8; ++k) {
      auto t = __builtin_amdgcn_permlane32_swap(__float_as_uint(a0[k >> 1][k & 1]), __float_as_uint(a1[k >> 1][k & 1]), false, false);
      o[k] = __uint_as_float(t[0]) + __uint_as_float(t[1]);
    }
    *reinterpret_cast<f32x4*>(sc + lane * 4) = f32x4{o[0], o[1], o[2], o[3]};
    *reinterpret_cast<f32x4*>(sc + lane * 4 + HALF2) = f32x4{o[4], o[5], o[6], o[7]};
    flag_bump(cw + C_DONE);
  }

}

}

extern "C" void kernel_launch(void* const* d_in, const int* in_sizes, int n_in, void* d_out, int out_size, void* d_ws,
                              size_t ws_size, hipStream_t stream) {
  gqa_decode<<<NGROUP * NSPLIT, NTHR, 0, stream>>>((const unsigned char*)d_in[0], (const unsigned char*)d_in[1],
                                                   (const unsigned char*)d_in[2], (unsigned char*)d_ws,
                                                   (unsigned char*)d_out);
}
```
